```python
import torch

def _softmax(x, axis=-1):
    return torch.softmax(x, dim=axis)
def _gelu(x):
    return torch.nn.functional.gelu(x, approximate="tanh")
def _silu(x):
    return torch.nn.functional.silu(x)
def _sigmoid(x):
    return torch.sigmoid(x)
def _layernorm(x, axis=-1, eps=1e-5):
    m = x.mean(dim=axis, keepdim=True)
    v = x.var(dim=axis, keepdim=True, unbiased=False)
    return (x - m) / torch.sqrt(v + eps)
def _scatter_add(tgt, idx, src, axis=0):
    return tgt.clone().index_add_(axis, idx, src)
def _segment_sum(src, seg, n):
    out = torch.zeros((n,) + src.shape[1:], dtype=src.dtype)
    return out.index_add_(0, seg, src)
def _pad(x, pw):
    flat = tuple(v for pair in reversed(pw) for v in pair)
    return torch.nn.functional.pad(x, flat)

kernel_name = 'sequential_preLN__linear_kernel_hedgehog__glu_gated_geglu__prompt'

def setup_inputs():

    _w = (lambda a: (lambda t: t.to(torch.bfloat16) if t.is_floating_point() else t)(torch.as_tensor(a)))
    def _A_setup():
        B, S, D, H = 1, 2048, 448, 16
        Dh = D // H
        return {
            "x": _w((lambda *s: torch.randn(*s, dtype=torch.bfloat16) * 0.05)(B, S, D)),
            "wqkv": _w((lambda *s: torch.randn(*s, dtype=torch.bfloat16) * 0.2)(D, 3 * D)),
            "w_phi": _w((lambda *s: torch.randn(*s, dtype=torch.bfloat16) * 0.2)(H, Dh, Dh) * (Dh ** -0.5)),
            "wo": _w((lambda *s: torch.randn(*s, dtype=torch.bfloat16) * 0.05)(D, D)),
        }
    _A_in = _A_setup()

    def _B_setup():
        B, S, Dm, Df = 1, 2048, 448, 1792
        return {
            "x": _w((lambda *s: torch.randn(*s, dtype=torch.bfloat16) * 0.05)(B, S, Dm)),
            "w_gate": _w((lambda *s: torch.randn(*s, dtype=torch.bfloat16) * 0.4)(Dm, Df) * (Dm ** -0.5)),
            "w_up": _w((lambda *s: torch.randn(*s, dtype=torch.bfloat16) * 0.4)(Dm, Df) * (Dm ** -0.5)),
            "w_down": _w((lambda *s: torch.randn(*s, dtype=torch.bfloat16) * 0.4)(Df, Dm) * (Df ** -0.5)),
        }
    _B_in = _B_setup()

    return {
        "A_x": _A_in["x"],
        "A_wqkv": _A_in["wqkv"],
        "A_w_phi": _A_in["w_phi"],
        "A_wo": _A_in["wo"],
        "B_x": _B_in["x"],
        "B_w_gate": _B_in["w_gate"],
        "B_w_up": _B_in["w_up"],
        "B_w_down": _B_in["w_down"],
    }

def reference(A_x, A_wqkv, A_w_phi, A_wo, B_x, B_w_gate, B_w_up, B_w_down):
    def _A_ref(x, wqkv, w_phi, wo):
        B, S, D = x.shape
        H = 16; Dh = D // H
        qkv = (lambda x, *d: x.permute(*d))((x @ wqkv).reshape(B, S, 3, H, Dh), 2, 0, 3, 1, 4)
        q, k, v = qkv[0], qkv[1], qkv[2]
        pq = torch.einsum("bhsd,hdf->bhsf", q, w_phi)
        pk = torch.einsum("bhsd,hdf->bhsf", k, w_phi)
        phi_q = torch.concatenate([_softmax(pq, axis=-1), _softmax(-pq, axis=-1)], axis=-1)
        phi_k = torch.concatenate([_softmax(pk, axis=-1), _softmax(-pk, axis=-1)], axis=-1)
        kv = torch.einsum("bhsf,bhsd->bhfd", phi_k, v)
        z  = torch.einsum("bhsf->bhf", phi_k)
        num = torch.einsum("bhqf,bhfd->bhqd", phi_q, kv)
        den = torch.einsum("bhqf,bhf->bhq", phi_q, z)[..., None] + 1e-6
        o = num / den
        return (lambda x, *d: x.permute(*d))(o, 0, 2, 1, 3).reshape(B, S, D) @ wo

    def _B_ref(x, w_gate, w_up, w_down):
        g = torch.einsum("bsd,df->bsf", x, w_gate)
        u = torch.einsum("bsd,df->bsf", x, w_up)
        h = _gelu(g) * u
        return torch.einsum("bsf,fd->bsd", h, w_down)

    x = A_x
    h = x + _A_ref(_layernorm(x, axis=-1), A_wqkv, A_w_phi, A_wo)
    h2 = h + _B_ref(_layernorm(h, axis=-1), B_w_gate, B_w_up, B_w_down)
    return h2
```

```cpp
#include <hip/hip_runtime.h>
#include <hip/hip_bf16.h>
typedef unsigned short u16;
__device__ __forceinline__ float b2f(u16 b){ return __uint_as_float(((unsigned)b)<<16); }
__device__ __forceinline__ u16 f2b(float f){ unsigned u=__float_as_uint(f); u += 0x7fffu + ((u>>16)&1u); return (u16)(u>>16); }
__device__ __forceinline__ float rb(float f){ return b2f(f2b(f)); }
constexpr int S=2048,D=448,H=16,DH=28,DF=1792;

__global__ void k_ln(const u16* x, u16* y){
  int r=blockIdx.x; int t=threadIdx.x;
  float v[7]; float s=0;
  for(int i=0;i<7;i++){ v[i]=b2f(x[r*D+t+64*i]); s+=v[i]; }
  for(int o=32;o;o>>=1) s+=__shfl_xor(s,o);
  float m=s/D; float q=0;
  for(int i=0;i<7;i++){ float d=v[i]-m; q+=d*d; }
  for(int o=32;o;o>>=1) q+=__shfl_xor(q,o);
  float rs=rsqrtf(q/D+1e-5f);
  for(int i=0;i<7;i++) y[r*D+t+64*i]=f2b((v[i]-m)*rs);
}
__global__ void k_gemm(const u16* A,const u16* B,u16* C,const u16* res,int M,int N,int K){
  int n=blockIdx.x*64+threadIdx.x; int m=blockIdx.y;
  if(n>=N) return;
  float acc=0;
  for(int k=0;k<K;k++) acc+=b2f(A[m*K+k])*b2f(B[k*N+n]);
  if(res) acc=rb(acc)+b2f(res[m*N+n]);
  C[m*N+n]=f2b(acc);
}
__global__ void k_phi(const u16* qkv,const u16* wphi,u16* phi){
  int idx=blockIdx.x*256+threadIdx.x; if(idx>=2*S*H) return;
  int w=idx/(S*H); int s=(idx/H)%S; int h=idx%H;
  float p[DH]; float mx=-1e30f, mn=1e30f;
  for(int f=0;f<DH;f++){ float a=0; for(int d=0;d<DH;d++) a+=b2f(qkv[s*3*D+w*D+h*DH+d])*b2f(wphi[(h*DH+d)*DH+f]); a=rb(a); p[f]=a; mx=fmaxf(mx,a); mn=fminf(mn,a);}
  float s1=0,s2=0; for(int f=0;f<DH;f++){ s1+=__expf(p[f]-mx); s2+=__expf(mn-p[f]); }
  u16* o=phi+(((size_t)w*S+s)*H+h)*56;
  for(int f=0;f<DH;f++){ o[f]=f2b(__expf(p[f]-mx)/s1); o[28+f]=f2b(__expf(mn-p[f])/s2);}
}
__global__ void k_kv(const u16* qkv,const u16* phi,float* kv){
  int idx=blockIdx.x*64+threadIdx.x; if(idx>=H*56*29) return;
  int h=idx/(56*29); int f=(idx/29)%56; int d=idx%29;
  const u16* pk=phi+(size_t)S*H*56;
  float a=0;
  for(int s=0;s<S;s++){ float v= d<28? b2f(qkv[s*3*D+2*D+h*DH+d]):1.f; a+=b2f(pk[(s*H+h)*56+f])*v; }
  kv[idx]=rb(a);
}
__global__ void k_o(const u16* phi,const float* kv,u16* o){
  int idx=blockIdx.x*256+threadIdx.x; if(idx>=S*D) return;
  int s=idx/D; int c=idx%D; int h=c/DH; int d=c%DH;
  float n=0,dn=0;
  for(int f=0;f<56;f++){ float p=b2f(phi[(s*H+h)*56+f]); n+=p*kv[(h*56+f)*29+d]; dn+=p*kv[(h*56+f)*29+28]; }
  o[idx]=f2b(rb(n)/rb(rb(dn)+1e-6f));
}
__global__ void k_act(const u16* g,const u16* u,u16* a){
  int idx=blockIdx.x*256+threadIdx.x; if(idx>=S*DF) return;
  float x=b2f(g[idx]); float t=tanhf(0.7978845608f*(x+0.044715f*x*x*x));
  a[idx]=f2b(rb(0.5f*x*(1.f+t))*b2f(u[idx]));
}
extern "C" void kernel_launch(void* const* d_in, const int* in_sizes, int n_in, void* d_out, int out_size, void* d_ws, size_t ws_size, hipStream_t stream){
  const u16* x=(const u16*)d_in[0]; const u16* wqkv=(const u16*)d_in[1]; const u16* wphi=(const u16*)d_in[2]; const u16* wo=(const u16*)d_in[3];
  const u16* wg=(const u16*)d_in[5]; const u16* wu=(const u16*)d_in[6]; const u16* wd=(const u16*)d_in[7];
  char* ws=(char*)d_ws; size_t MB=1<<20;
  u16* xn=(u16*)(ws); u16* qkv=(u16*)(ws+8*MB); u16* phi=(u16*)(ws+16*MB); float* kv=(float*)(ws+32*MB); u16* o=(u16*)(ws+40*MB);
  u16* hh=(u16*)(ws+48*MB); u16* hn=(u16*)(ws+56*MB); u16* g=(u16*)(ws+64*MB); u16* u=(u16*)(ws+72*MB); u16* a=(u16*)(ws+80*MB);
  k_ln<<<S,64,0,stream>>>(x,xn);
  k_gemm<<<dim3(21,S),64,0,stream>>>(xn,wqkv,qkv,nullptr,S,3*D,D);
  k_phi<<<2*S*H/256,256,0,stream>>>(qkv,wphi,phi);
  k_kv<<<(H*56*29+63)/64,64,0,stream>>>(qkv,phi,kv);
  k_o<<<S*D/256,256,0,stream>>>(phi,kv,o);
  k_gemm<<<dim3(7,S),64,0,stream>>>(o,wo,hh,x,S,D,D);
  k_ln<<<S,64,0,stream>>>(hh,hn);
  k_gemm<<<dim3(28,S),64,0,stream>>>(hn,wg,g,nullptr,S,DF,D);
  k_gemm<<<dim3(28,S),64,0,stream>>>(hn,wu,u,nullptr,S,DF,D);
  k_act<<<S*DF/256,256,0,stream>>>(g,u,a);
  k_gemm<<<dim3(7,S),64,0,stream>>>(a,wd,(u16*)d_out,hh,S,D,DF);
}
```

```cpp
#include <hip/hip_runtime.h>

namespace hk {
typedef unsigned short u16;
typedef unsigned v4u __attribute__((ext_vector_type(4)));
typedef unsigned v2u __attribute__((ext_vector_type(2)));
typedef float f32x16 __attribute__((ext_vector_type(16)));
typedef float f32x4 __attribute__((ext_vector_type(4)));
typedef float f32x2 __attribute__((ext_vector_type(2)));
typedef __bf16 bf16x8 __attribute__((ext_vector_type(8)));
typedef __bf16 bf16x2 __attribute__((ext_vector_type(2)));
typedef short s16x4 __attribute__((ext_vector_type(4)));
#define GAS __attribute__((address_space(1)))
#define LAS __attribute__((address_space(3)))
#define DI __device__ __forceinline__
#define SCHED() __builtin_amdgcn_sched_barrier(0)

constexpr int SEQ = 2048, DM = 448, NH = 16, DH = 28, DFF = 1792;
constexpr int OFF_CTRL = 0;
constexpr int OFF_WA = 65536;
constexpr int OFF_WO = OFF_WA + 16 * 3 * 28 * 1024;
constexpr int OFF_WC = OFF_WO + 28 * 16 * 1024;
constexpr int OFF_WD = OFF_WC + 112 * 28 * 1024;
constexpr int OFF_KVP = OFF_WD + 14 * 112 * 1024;
constexpr int OFF_O = OFF_KVP + 16 * 16 * 4096;
constexpr int OFF_H = OFF_O + 64 * 16 * 2048;
constexpr int OFF_ACT = OFF_H + 64 * 28 * 1024;
constexpr int OFF_END = OFF_ACT + 64 * 112 * 1024;
constexpr int NFRAG_W = (OFF_KVP - OFF_WA) / 1024;
constexpr int NFRAG_X = (OFF_END - OFF_KVP) / 1024;
constexpr unsigned SENT = 0xFFFFFFFFu;
constexpr unsigned SPIN_CAP = 1u << 15;
constexpr int AUX_SC1 = 16;

constexpr int L_X = 0;
constexpr int L_WK = 114688;
constexpr int L_WV = 0, L_WQ = 28672;
constexpr int L_PHI = 57344;
constexpr int L_V = L_PHI + 128 * 136;
constexpr int L_KV = L_V + 128 * 72;
constexpr int L_OIMG = L_KV + 4096;
constexpr int L_SM = 143360;
constexpr int L_WPHI = L_SM;
constexpr int L_CS = L_WPHI + 2048;
constexpr int L_COR = L_CS + 3 * 32 * 80;
constexpr int L_JUNK = L_COR + 4 * 32 * 32;
constexpr int L_END = L_JUNK + 1024;
static_assert(L_END <= 163840, "");
constexpr int L_H = 0;
constexpr int L_ST2 = 114688;
constexpr int L_RED = 0;
constexpr int LDS_BYTES = 163840;

DI float b2f(u16 b) { return __uint_as_float(((unsigned)b) << 16); }
DI unsigned pk2(float a, float b) { f32x2 v = {a, b}; return __builtin_bit_cast(unsigned, __builtin_convertvector(v, bf16x2)); }
DI float lo_f(unsigned u) { return __uint_as_float(u << 16); }
DI float hi_f(unsigned u) { return __uint_as_float(u & 0xffff0000u); }
DI float dot2(unsigned a, unsigned b, float c) { return __builtin_amdgcn_fdot2_f32_bf16(__builtin_bit_cast(bf16x2, a), __builtin_bit_cast(bf16x2, b), c, false); }
DI f32x16 mfma32(v4u a, v4u b, f32x16 c) { return __builtin_amdgcn_mfma_f32_32x32x16_bf16(__builtin_bit_cast(bf16x8, a), __builtin_bit_cast(bf16x8, b), c, 0, 0, 0); }
DI f32x4 mfma16(v4u a, v4u b, f32x4 c) { return __builtin_amdgcn_mfma_f32_16x16x32_bf16(__builtin_bit_cast(bf16x8, a), __builtin_bit_cast(bf16x8, b), c, 0, 0, 0); }
DI void bar() { asm volatile("s_waitcnt lgkmcnt(0)" ::: "memory"); __builtin_amdgcn_s_barrier(); asm volatile("" ::: "memory"); }
DI float swap_other(float v) {
  unsigned u = __float_as_uint(v);
  auto r = __builtin_amdgcn_permlane32_swap(u, u, false, false);
  return __uint_as_float(r[0] ^ r[1] ^ u);
}
DI float xsum(float v) { unsigned u = __float_as_uint(v); auto r = __builtin_amdgcn_permlane32_swap(u, u, false, false); return __uint_as_float(r[0]) + __uint_as_float(r[1]); }
DI float xmax(float v) { unsigned u = __float_as_uint(v); auto r = __builtin_amdgcn_permlane32_swap(u, u, false, false); return fmaxf(__uint_as_float(r[0]), __uint_as_float(r[1])); }
DI float xmin(float v) { unsigned u = __float_as_uint(v); auto r = __builtin_amdgcn_permlane32_swap(u, u, false, false); return fminf(__uint_as_float(r[0]), __uint_as_float(r[1])); }
DI unsigned guard(unsigned u) { return u < 0xFFFFFFFEu ? u : 0xFFFFFFFEu; }
DI v4u guard4(v4u v) { return v4u{guard(v.x), guard(v.y), guard(v.z), guard(v.w)}; }
DI unsigned mx4(unsigned m, v4u a) { m = max(m, max(a.x, a.y)); return max(m, max(a.z, a.w)); }
typedef __amdgpu_buffer_rsrc_t rsrc_t;
DI v4u ldc(rsrc_t rs, int voff, int soff) { return __builtin_amdgcn_raw_buffer_load_b128(rs, voff, soff, AUX_SC1); }
DI void stc(rsrc_t rs, int voff, int soff, v4u v) { __builtin_amdgcn_raw_buffer_store_b128(v, rs, voff, soff, AUX_SC1); }
DI v4u lds128(LAS char* L, int off) { return *(LAS v4u*)(L + off); }
DI v2u ldtr(LAS char* L, int off) { return __builtin_bit_cast(v2u, __builtin_amdgcn_ds_read_tr16_b64_v4i16((LAS s16x4*)(L + off))); }
DI v4u pack8(const f32x16& a, int s) { return v4u{pk2(a[8 * s], a[8 * s + 1]), pk2(a[8 * s + 2], a[8 * s + 3]), pk2(a[8 * s + 4], a[8 * s + 5]), pk2(a[8 * s + 6], a[8 * s + 7])}; }

template <int NK, int P, bool REG_IS_A> DI f32x16 gemm_lds(const v4u (&reg)[NK], LAS char* L, int base, f32x16 acc) {
  v4u buf[P];
#pragma unroll
  for (int i = 0; i < P; ++i) buf[i] = lds128(L, base + i * 1024);
  __builtin_amdgcn_s_setprio(2);
  SCHED();
#pragma unroll
  for (int ks = 0; ks < NK; ++ks) {
    acc = REG_IS_A ? mfma32(reg[ks], buf[ks % P], acc) : mfma32(buf[ks % P], reg[ks], acc);
    if (ks + P < NK) buf[ks % P] = lds128(L, base + (ks + P) * 1024);
    SCHED();
  }
  __builtin_amdgcn_s_setprio(0);
  return acc;
}
template <int N, int STRIDE> DI void issue(v4u (&a)[N], rsrc_t rs, int voff, int soff) {
#pragma unroll
  for (int i = 0; i < N; ++i) a[i] = ldc(rs, voff, soff + i * STRIDE);
}
template <int N, int STRIDE> DI void ensure(v4u (&a)[N], rsrc_t rs, int voff, int soff) {
  unsigned m = 0;
#pragma unroll
  for (int i = 0; i < N; ++i) m = mx4(m, a[i]);
  if (__builtin_expect(__any(m == SENT), 0)) {
    for (unsigned sp = 0; sp < SPIN_CAP; ++sp) {
      asm volatile("" ::: "memory");
      issue<N, STRIDE>(a, rs, voff, soff);
      m = 0;
#pragma unroll
      for (int i = 0; i < N; ++i) m = mx4(m, a[i]);
      if (!__any(m == SENT)) break;
    }
  }
}
DI void delay(int n) {
  for (int i = 0; i < n; ++i) __builtin_amdgcn_s_sleep(1);
}

#define ST(k)

struct Args { const u16 *x, *wqkv, *wphi, *wo, *wg, *wu, *wd; u16* out; char* ws; };

__global__ void __launch_bounds__(256) k_prep(Args a) {
  int idx = blockIdx.x * 256 + threadIdx.x;
  int f = idx >> 6, l = idx & 63;
  v4u o;
  if (f >= NFRAG_W) { o = v4u{SENT, SENT, SENT, SENT}; }
  else {
    u16 e[8];
    int r = l & 31, hh = l >> 5;
    if (f < 1344) {
      int h = f / 84, p = (f / 28) % 3, ks = f % 28;
      int col = (p == 0 ? 448 : p == 1 ? 896 : 0) + h * 28 + r;
      for (int j = 0; j < 8; ++j) e[j] = r < 28 ? a.wqkv[(16 * ks + 8 * hh + j) * 1344 + col] : (u16)0;
    } else if (f < 1344 + 448) {
      int g = f - 1344, nt = g / 16, h = g % 16; int c = l & 15, gg = l >> 4;
      for (int j = 0; j < 8; ++j) { int d = 16 * (gg >> 1) + 8 * (j >> 2) + 4 * (gg & 1) + (j & 3); e[j] = d < 28 ? a.wo[(28 * h + d) * 448 + 16 * nt + c] : (u16)0; }
    } else if (f < 1344 + 448 + 3136) {
      int g = f - 1344 - 448, T = g / 28, ks = g % 28;
      const u16* w = r < 16 ? a.wg : a.wu;
      for (int j = 0; j < 8; ++j) e[j] = w[(16 * ks + 8 * (j >> 2) + 4 * hh + (j & 3)) * 1792 + 16 * T + (r & 15)];
    } else {
      int g = f - 1344 - 448 - 3136, nt = g / 112, T = g % 112;
      for (int j = 0; j < 8; ++j) e[j] = a.wd[(16 * T + 8 * (j >> 2) + 4 * hh + (j & 3)) * 448 + 32 * nt + r];
    }
    o = guard4(v4u{e[0] | (unsigned)e[1] << 16, e[2] | (unsigned)e[3] << 16, e[4] | (unsigned)e[5] << 16, e[6] | (unsigned)e[7] << 16});
  }
  *(v4u*)(a.ws + OFF_WA + (size_t)idx * 16) = o;
}

constexpr float L2E = 1.4426950408889634f;

#define SUM4(a, v) do { a.x = dot2(v.x, ONES, a.x); a.y = dot2(v.y, ONES, a.y); a.z = dot2(v.z, ONES, a.z); a.w = dot2(v.w, ONES, a.w); } while (0)
#define SQ4(a, v) do { a.x = dot2(v.x, v.x, a.x); a.y = dot2(v.y, v.y, a.y); a.z = dot2(v.z, v.z, a.z); a.w = dot2(v.w, v.w, a.w); } while (0)
DI unsigned hilo(float v) { unsigned h = pk2(v, 0.f) & 0xffffu; float r = v - lo_f(h); return h | (pk2(r, 0.f) << 16); }

DI void feature_map(const f32x16& acc, float rstd, const v4u (&wp)[2], bool hh, f32x16& ep, f32x16& en) {
  f32x16 t = acc * rstd;
  f32x16 p = {};
  p = mfma32(wp[0], pack8(t, 0), p);
  p = mfma32(wp[1], pack8(t, 1), p);
  float mx = fmaxf(fmaxf(p[12], p[13]), fmaxf(p[14], p[15])), mn = fminf(fminf(p[12], p[13]), fminf(p[14], p[15]));
  mx = hh ? -__builtin_inff() : mx; mn = hh ? __builtin_inff() : mn;
#pragma unroll
  for (int i = 0; i < 12; i += 2) { mx = fmaxf(fmaxf(mx, p[i]), p[i + 1]); mn = fminf(fminf(mn, p[i]), p[i + 1]); }
  mx = xmax(mx); mn = xmin(mn);
  float bp = -mx * L2E, bn = mn * L2E;
  float bpp = hh ? -__builtin_inff() : bp, bnp = hh ? -__builtin_inff() : bn;
  float sp = 0.f, sn = 0.f;
#pragma unroll
  for (int i = 0; i < 16; ++i) {
    ep[i] = __builtin_amdgcn_exp2f(__builtin_fmaf(p[i], L2E, i < 12 ? bp : bpp));
    en[i] = __builtin_amdgcn_exp2f(__builtin_fmaf(p[i], -L2E, i < 12 ? bn : bnp));
    sp += ep[i]; sn += en[i];
  }
  float rp = __builtin_amdgcn_rcpf(xsum(sp)), rn = __builtin_amdgcn_rcpf(xsum(sn));
  ep *= rp; en *= rn;
}

__global__ void __launch_bounds__(512, 2) k_main(Args a) {
  extern __shared__ __attribute__((aligned(16))) char smem[];
  LAS char* L = (LAS char*)smem;
  const int b = blockIdx.x, xcd = b & 7, ci = b >> 3;
  const int tid = threadIdx.x, lane = tid & 63, w = __builtin_amdgcn_readfirstlane(tid >> 6);
  const int r32 = lane & 31; const bool hh = lane >= 32;
  const rsrc_t rs = __builtin_amdgcn_make_buffer_rsrc(a.ws, 0, OFF_END, 0x00020000);
  const int l16 = lane * 16;
  unsigned ONES, SEL_LO, SEL_HI;
  asm volatile("s_mov_b32 %0, 0x3f803f80\n\ts_mov_b32 %1, 0x3f80\n\ts_mov_b32 %2, 0x3f800000" : "=s"(ONES), "=s"(SEL_LO), "=s"(SEL_HI));
  ST(0);
  {
    unsigned long long pc; unsigned len;
    asm volatile("s_getpc_b64 %0\nhk_code_begin:\n\ts_mov_b32 %1, hk_code_end-hk_code_begin" : "=s"(pc), "=s"(len));
    if (w == 7) {
      unsigned off = ((ci & 3) * 64 + lane) * 128;
      off = off < len ? off : 0u;
      __builtin_amdgcn_global_load_lds((const GAS void*)((const GAS char*)pc + off), (LAS void*)(L + L_JUNK), 4, 0, 0);
    }
  }
  const int tbA = 8 * (xcd & 1) + (ci >> 2), hA = 4 * (xcd >> 1) + (ci & 3);
  const int t4 = w & 3;
  {
    const int row0 = w * 16 + (lane >> 3);
    const char* xp0 = (const char*)a.x + (size_t)(tbA * 128 + row0) * 896 + (((lane & 7) ^ ((row0 >> 1) & 7)) * 16);
    const char* xp1 = (const char*)a.x + (size_t)(tbA * 128 + row0 + 8) * 896 + (((lane & 7) ^ (((row0 + 8) >> 1) & 7)) * 16);
#pragma unroll
    for (int sg = 0; sg < 7; ++sg) {
      __builtin_amdgcn_global_load_lds((const GAS void*)(xp0 + sg * 128), (LAS void*)(L + L_X + sg * 16384 + w * 2048), 16, 0, 0);
      __builtin_amdgcn_global_load_lds((const GAS void*)(xp1 + sg * 128), (LAS void*)(L + L_X + sg * 16384 + w * 2048 + 1024), 16, 0, 0);
    }
    if (w < 2) {
      int c = w * 64 + lane; c = c < 98 ? c : 97;
      __builtin_amdgcn_global_load_lds((const GAS void*)((const char*)a.wphi + hA * 1568 + c * 16), (LAS void*)(L + L_WPHI + w * 1024), 16, 0, 0);
    }
  }
  v4u wk[4], wv[4], wq[4];
  const int waoff = OFF_WA + hA * 84 * 1024;
  const int wlast = (w < 4 ? 24 + w : 27) * 1024 - (24 + w) * 1024;
  (void)wlast;
  const int wvo = l16 + w * 1024;
  const int wvo3 = l16 + (w < 4 ? w : 3) * 1024;
#define ISSUE_PART(arr, p) do { arr[0] = ldc(rs, wvo, waoff + (p) * 28672); arr[1] = ldc(rs, wvo, waoff + (p) * 28672 + 8192); arr[2] = ldc(rs, wvo, waoff + (p) * 28672 + 16384); arr[3] = ldc(rs, wvo3, waoff + (p) * 28672 + 24576); } while (0)
  ISSUE_PART(wk, 0); ISSUE_PART(wv, 1); ISSUE_PART(wq, 2);
  SCHED();
  asm volatile("s_waitcnt vmcnt(12)" ::: "memory");
  bar();
  SCHED();
  ST(1);
  v4u xf[28];
  {
    const int row = 32 * t4 + r32; const int key = (row >> 1) & 7;
#pragma unroll
    for (int ks = 0; ks < 28; ++ks) xf[ks] = lds128(L, L_X + (ks >> 2) * 16384 + row * 128 + (((2 * (ks & 3) + (hh ? 1 : 0)) ^ key) * 16));
  }
  v4u wp[2];
  {
    const int q = (lane & 15) >> 2, pp = lane & 3, blk = (lane >> 4) & 1;
#pragma unroll
    for (int s = 0; s < 2; ++s) {
      v2u lo = ldtr(L, L_WPHI + (16 * s + 4 * (hh ? 1 : 0) + q) * 56 + blk * 32 + pp * 8);
      v2u hi = ldtr(L, L_WPHI + (16 * s + 8 + 4 * (hh ? 1 : 0) + q) * 56 + blk * 32 + pp * 8);
      if (s == 1 && hh) hi = v2u{0, 0};
      if (r32 >= 28) { lo = v2u{0, 0}; hi = v2u{0, 0}; }
      wp[s] = v4u{lo.x, lo.y, hi.x, hi.y};
    }
  }
#define STAGE_PART(arr, p, LB) do { f32x4 c_ = {}; \
    _Pragma("unroll") for (int i = 0; i < 4; ++i) { if (i < 3 || w < 4) { *(LAS v4u*)(L + (LB) + i * 8192 + wvo) = arr[i]; SUM4(c_, arr[i]); } } \
    *(LAS float*)(L + L_CS + (p) * 2560 + r32 * 80 + (hh ? 32 : 0) + w * 4) = (c_.x + c_.y) + (c_.z + c_.w); } while (0)
  STAGE_PART(wk, 0, L_WK);
  bar();
  ST(2);
  STAGE_PART(wv, 1, L_WV); STAGE_PART(wq, 2, L_WQ);
  auto cs_frag = [&](int p) -> v4u {
    f32x4 c0 = *(LAS f32x4*)(L + L_CS + p * 2560 + r32 * 80 + (hh ? 32 : 0)), c1 = *(LAS f32x4*)(L + L_CS + p * 2560 + r32 * 80 + (hh ? 32 : 0) + 16);
    c0 += c1; float c = xsum((c0.x + c0.y) + (c0.z + c0.w));
    unsigned hl = hh ? 0u : hilo(c);
    return v4u{hl, hl, 0u, 0u};
  };
  v4u phq[4];
  float rstd = 0.f; v4u mfr = {0, 0, 0, 0};
  f32x16 acc = {};
  if (w < 4) {
    acc = gemm_lds<28, 6, false>(xf, L, L_WK + l16, acc);
  } else {
    f32x4 a1 = {}, a2 = {};
#pragma unroll
    for (int ks = 0; ks < 28; ++ks) { SUM4(a1, xf[ks]); SQ4(a2, xf[ks]); }
    float s1 = (a1.x + a1.y) + (a1.z + a1.w), s2 = (a2.x + a2.y) + (a2.z + a2.w);
    float mean = xsum(s1) * (1.f / DM), var = fmaxf(xsum(s2) * (1.f / DM) - mean * mean, 0.f);
    rstd = __builtin_amdgcn_rsqf(var + 1e-5f);
    unsigned hl = hilo(-mean);
    unsigned mh = hh ? 0u : (hl & 0xffffu) * 0x10001u, ml = hh ? 0u : (hl >> 16) * 0x10001u;
    mfr = v4u{mh, ml, 0u, 0u};
    v4u ck = cs_frag(0);
    if (!hh) { *(LAS v4u*)(L + L_COR + t4 * 1024 + r32 * 32) = v4u{ck.x, mh, ml, __float_as_uint(rstd)}; }
  }
  bar();
  ST(3);
  if (w < 4) {
    v4u cr = lds128(L, L_COR + t4 * 1024 + r32 * 32);
    rstd = __uint_as_float(cr.w);
    v4u ca = {hh ? 0u : cr.x, hh ? 0u : cr.x, 0u, 0u}, cb = {hh ? 0u : cr.y, hh ? 0u : cr.z, 0u, 0u};
    acc = mfma32(ca, cb, acc);
    f32x16 ep, en;
    feature_map(acc, rstd, wp, hh, ep, en);
    const int ro = L_PHI + (32 * t4 + r32) * 136 + (hh ? 8 : 0);
#pragma unroll
    for (int g = 0; g < 4; ++g) {
      *(LAS v2u*)(L + ro + g * 16) = v2u{pk2(ep[4 * g], ep[4 * g + 1]), pk2(ep[4 * g + 2], ep[4 * g + 3])};
      *(LAS v2u*)(L + ro + 64 + g * 16) = v2u{pk2(en[4 * g], en[4 * g + 1]), pk2(en[4 * g + 2], en[4 * g + 3])};
    }
  } else {
    acc = gemm_lds<28, 6, false>(xf, L, L_WV + l16, acc);
    acc = mfma32(cs_frag(1), mfr, acc);
    acc *= rstd;
    if (hh) acc[12] = 1.0f;
    const int ro = L_V + (32 * t4 + r32) * 72 + (hh ? 8 : 0);
#pragma unroll
    for (int g = 0; g < 4; ++g) *(LAS v2u*)(L + ro + g * 16) = v2u{pk2(acc[4 * g], acc[4 * g + 1]), pk2(acc[4 * g + 2], acc[4 * g + 3])};
  }
  bar();
  ST(4);
  if (w < 4) {
    if (w < 2) {
      const int q = (lane & 15) >> 2, pp = lane & 3, blk = (lane >> 4) & 1;
      f32x16 z = {};
      const int ta = L_PHI + ((hh ? 8 : 0) + q) * 136 + w * 64 + blk * 32 + pp * 8;
      const int tv = L_V + ((hh ? 8 : 0) + q) * 72 + blk * 32 + pp * 8;
      v2u a0[8], a1[8], b0[8], b1[8];
#pragma unroll
      for (int s = 0; s < 8; ++s) {
        a0[s] = ldtr(L, ta + s * 16 * 136); a1[s] = ldtr(L, ta + (s * 16 + 4) * 136);
        b0[s] = ldtr(L, tv + s * 16 * 72); b1[s] = ldtr(L, tv + (s * 16 + 4) * 72);
      }
      SCHED();
#pragma unroll
      for (int s = 0; s < 8; ++s) z = mfma32(v4u{a0[s].x, a0[s].y, a1[s].x, a1[s].y}, v4u{b0[s].x, b0[s].y, b1[s].x, b1[s].y}, z);
      const int ko = OFF_KVP + (hA * 16 + tbA) * 4096 + w * 2048;
      stc(rs, l16, ko, guard4(pack8(z, 0)));
      stc(rs, l16, ko + 1024, guard4(pack8(z, 1)));
    }
    ST(5);
    delay(w < 2 ? 8 : 14);
    v4u kp[16];
    const int ko = OFF_KVP + hA * 16 * 4096 + w * 1024;
    issue<16, 4096>(kp, rs, l16, ko);
    ensure<16, 4096>(kp, rs, l16, ko);
    float sm[8] = {0.f, 0.f, 0.f, 0.f, 0.f, 0.f, 0.f, 0.f};
#pragma unroll
    for (int i = 0; i < 16; ++i) {
      sm[0] = dot2(kp[i].x, SEL_LO, sm[0]); sm[1] = dot2(kp[i].x, SEL_HI, sm[1]); sm[2] = dot2(kp[i].y, SEL_LO, sm[2]); sm[3] = dot2(kp[i].y, SEL_HI, sm[3]);
      sm[4] = dot2(kp[i].z, SEL_LO, sm[4]); sm[5] = dot2(kp[i].z, SEL_HI, sm[5]); sm[6] = dot2(kp[i].w, SEL_LO, sm[6]); sm[7] = dot2(kp[i].w, SEL_HI, sm[7]);
    }
    *(LAS v4u*)(L + L_KV + w * 1024 + l16) = v4u{pk2(sm[0], sm[1]), pk2(sm[2], sm[3]), pk2(sm[4], sm[5]), pk2(sm[6], sm[7])};
  } else {
    acc = f32x16{};
    acc = gemm_lds<28, 6, false>(xf, L, L_WQ + l16, acc);
    acc = mfma32(cs_frag(2), mfr, acc);
    f32x16 ep, en;
    feature_map(acc, rstd, wp, hh, ep, en);
    phq[0] = pack8(ep, 0); phq[1] = pack8(ep, 1); phq[2] = pack8(en, 0); phq[3] = pack8(en, 1);
    ST(5);
  }
  ST(6);
  const int ttB = 8 * xcd + (ci >> 2), cgB = ci & 3;
  const int ntB = 7 * cgB + (w < 7 ? w : 6);
  v4u wo[16];
  issue<16, 1024>(wo, rs, l16, OFF_WO + ntB * 16384);
  v2u xr[2];
  {
    const u16* xp = a.x + (size_t)(32 * ttB + (lane & 15)) * DM + 16 * ntB + 4 * (lane >> 4);
    xr[0] = *(const GAS v2u*)xp; xr[1] = *(const GAS v2u*)(xp + 16 * DM);
  }
  SCHED();
  bar();
  ST(7);
  if (w >= 4) {
    f32x16 o = {};
#pragma unroll
    for (int s = 0; s < 4; ++s) o = mfma32(lds128(L, L_KV + s * 1024 + l16), phq[s], o);
    float dn = o[12]; { float ot = swap_other(dn); dn = hh ? dn : ot; }
    float rd = __builtin_amdgcn_rcpf(dn + 1e-6f);
    o *= rd;
    v4u o0 = pack8(o, 0), o1 = pack8(o, 1);
    if (hh) { o1.z = 0; o1.w = 0; }
    const int oo = OFF_O + ((tbA * 4 + t4) * 16 + hA) * 2048;
    stc(rs, l16, oo, guard4(o0)); stc(rs, l16, oo + 1024, guard4(o1));
  }
  ST(8);
  const bool actC = ci < 28;
  const int cc = actC ? ci : 27;
  const int tbC = 4 * (xcd >> 1) + cc / 7, jj = cc % 7, eC = xcd & 1;
  const int TC = 8 * (7 * eC + jj) + w;
  v4u wc[28];
  delay(w >= 4 ? 8 : 12);
  {
    v4u of[4];
    const int oo = OFF_O + ttB * 32768 + w * 4096;
    issue<4, 1024>(of, rs, l16, oo);
    SCHED();
    issue<28, 1024>(wc, rs, l16, OFF_WC + TC * 28672);
    SCHED();
    ensure<4, 1024>(of, rs, l16, oo);
#pragma unroll
    for (int i = 0; i < 4; ++i) *(LAS v4u*)(L + L_OIMG + w * 4096 + i * 1024 + l16) = of[i];
    bar();
    ST(9);
    if (w < 7) {
      const int c = lane & 15, g = lane >> 4;
      const int bo = L_OIMG + (g >> 1) * 1024 + ((g & 1) * 32 + c) * 16;
      f32x4 h0 = {}, h1 = {};
      v4u ob[8];
#pragma unroll
      for (int i = 0; i < 8; ++i) ob[i] = lds128(L, bo + (i >> 1) * 2048 + (i & 1) * 256);
      SCHED();
#pragma unroll
      for (int i = 0; i < 32; ++i) {
        if (i & 1) h1 = mfma16(wo[i >> 1], ob[i % 8], h1); else h0 = mfma16(wo[i >> 1], ob[i % 8], h0);
        if (i + 8 < 32) ob[i % 8] = lds128(L, bo + ((i + 8) >> 1) * 2048 + (i & 1) * 256);
        SCHED();
      }
      unsigned a0 = pk2(h0.x + lo_f(xr[0].x), h0.y + hi_f(xr[0].x)), a1 = pk2(h0.z + lo_f(xr[0].y), h0.w + hi_f(xr[0].y));
      unsigned b0 = pk2(h1.x + lo_f(xr[1].x), h1.y + hi_f(xr[1].x)), b1 = pk2(h1.z + lo_f(xr[1].y), h1.w + hi_f(xr[1].y));
      auto s0 = __builtin_amdgcn_permlane32_swap(a0, b0, false, false);
      auto s1 = __builtin_amdgcn_permlane32_swap(a1, b1, false, false);
      const int slot = (g & 1) * 32 + (hh ? 16 : 0) + c;
      stc(rs, slot * 16, OFF_H + (ttB * 28 + ntB) * 1024, guard4(v4u{s0[0], s1[0], s0[1], s1[1]}));
    }
  }
  ST(10);
  if (actC) {
    v4u csf;
    {
      f32x4 c4 = {};
#pragma unroll
      for (int ks = 0; ks < 28; ++ks) SUM4(c4, wc[ks]);
      float c_ = xsum((c4.x + c4.y) + (c4.z + c4.w));
      unsigned hl = hh ? 0u : hilo(c_);
      csf = v4u{hl, hl, 0u, 0u};
    }
    SCHED();
    v4u ha[7], hb[7];
    const int fo = ((w >> 2) * 28 + 7 * t4) * 1024;
    const int ho = OFF_H + tbC * 114688 + fo;
    issue<7, 1024>(ha, rs, l16, ho);
    issue<7, 1024>(hb, rs, l16, ho + 57344);
    SCHED();
    bar();
#define STAGE_H(arr, half) do { ensure<7, 1024>(arr, rs, l16, ho + (half) * 57344); f32x4 a1 = {}, a2 = {}; \
    _Pragma("unroll") for (int i = 0; i < 7; ++i) { *(LAS v4u*)(L + L_H + (half) * 57344 + fo + i * 1024 + l16) = arr[i]; SUM4(a1, arr[i]); SQ4(a2, arr[i]); } \
    float s1 = (a1.x + a1.y) + (a1.z + a1.w), s2 = (a2.x + a2.y) + (a2.z + a2.w); \
    *(LAS f32x2*)(L + L_ST2 + ((half) * 2 + (w >> 2)) * 2560 + r32 * 80 + (hh ? 32 : 0) + t4 * 8) = f32x2{s1, s2}; } while (0)
    STAGE_H(ha, 0);
    STAGE_H(hb, 1);
    bar();
    ST(11);
#pragma unroll 1
    for (int t = 0; t < 4; ++t) {
      f32x4 p0 = *(LAS f32x4*)(L + L_ST2 + t * 2560 + r32 * 80 + (hh ? 32 : 0)), p1 = *(LAS f32x4*)(L + L_ST2 + t * 2560 + r32 * 80 + (hh ? 32 : 0) + 16);
      p0 += p1;
      float mean = xsum(p0.x + p0.z) * (1.f / DM), var = fmaxf(xsum(p0.y + p0.w) * (1.f / DM) - mean * mean, 0.f) + 1e-5f;
      float rr = __builtin_amdgcn_rsqf(var);
      unsigned hl = hilo(-mean);
      v4u mf = {hh ? 0u : (hl & 0xffffu) * 0x10001u, hh ? 0u : (hl >> 16) * 0x10001u, 0u, 0u};
      f32x16 c = {};
      c = mfma32(csf, mf, c);
      const int hbase = L_H + t * 28672 + l16;
      c = gemm_lds<28, 6, true>(wc, L, hbase, c);
      if (t == 0) ST(16); if (t == 2) ST(17); if (t == 3) ST(18);
      const float k1 = -2.f * 0.7978845608028654f * L2E * rr, k2 = k1 * 0.044715f * rr * rr;
      float ov[8];
#pragma unroll
      for (int i = 0; i < 8; ++i) {
        float g = c[i], u = c[i + 8];
        float z = __builtin_fmaf(k2, g * g, k1) * g;
        float e = __builtin_amdgcn_exp2f(z);
        float rc = __builtin_amdgcn_rcpf(__builtin_fmaf(e, var, var));
        ov[i] = g * u * rc;
      }
      stc(rs, l16, OFF_ACT + ((tbC * 4 + t) * 112 + TC) * 1024, guard4(v4u{pk2(ov[0], ov[1]), pk2(ov[2], ov[3]), pk2(ov[4], ov[5]), pk2(ov[6], ov[7])}));
    }
    ST(12);
    v4u wd0[14], wd1[14];
    issue<14, 1024>(wd0, rs, l16, OFF_WD + ((2 * jj) * 112 + 14 * w) * 1024);
    issue<14, 1024>(wd1, rs, l16, OFF_WD + ((2 * jj + 1) * 112 + 14 * w) * 1024);
    const int tE = eC ? 1 : 0, tL = eC ? 2 : 3;
    const int rt = tid >> 8, tok = (tid >> 3) & 31, c8 = tid & 7;
    v2u hr0, hr1;
    {
      const int o_ = L_H + (rt ? tL : tE) * 28672 + (4 * jj + (c8 >> 1)) * 1024 + tok * 16 + (c8 & 1) * 8;
      hr0 = *(LAS v2u*)(L + o_); hr1 = *(LAS v2u*)(L + o_ + 512);
    }
    bar();
#pragma unroll
    for (int ph = 0; ph < 2; ++ph) {
      v4u af[14];
      const int ao = OFF_ACT + ((tbC * 4 + (ph ? tL : tE)) * 112 + 14 * w) * 1024;
      issue<14, 1024>(af, rs, l16, ao);
      ensure<14, 1024>(af, rs, l16, ao);
      f32x16 d0 = {}, d1 = {};
#pragma unroll
      for (int n = 0; n < 14; ++n) { d0 = mfma32(wd0[n], af[n], d0); d1 = mfma32(wd1[n], af[n], d1); }
      const int ro = L_RED + (ph * 8 + w) * 4608 + r32 * 144 + (hh ? 8 : 0);
#pragma unroll
      for (int g = 0; g < 4; ++g) {
        *(LAS v2u*)(L + ro + g * 16) = v2u{pk2(d0[4 * g], d0[4 * g + 1]), pk2(d0[4 * g + 2], d0[4 * g + 3])};
        *(LAS v2u*)(L + ro + 64 + g * 16) = v2u{pk2(d1[4 * g], d1[4 * g + 1]), pk2(d1[4 * g + 2], d1[4 * g + 3])};
      }
      ST(13 + ph);
    }
    bar();
    {
      float sm[8] = {lo_f(hr0.x), hi_f(hr0.x), lo_f(hr0.y), hi_f(hr0.y), lo_f(hr1.x), hi_f(hr1.x), lo_f(hr1.y), hi_f(hr1.y)};
#pragma unroll
      for (int p = 0; p < 8; ++p) {
        v4u v = lds128(L, L_RED + (rt * 8 + p) * 4608 + tok * 144 + c8 * 16);
        sm[0] = dot2(v.x, SEL_LO, sm[0]); sm[1] = dot2(v.x, SEL_HI, sm[1]); sm[2] = dot2(v.y, SEL_LO, sm[2]); sm[3] = dot2(v.y, SEL_HI, sm[3]);
        sm[4] = dot2(v.z, SEL_LO, sm[4]); sm[5] = dot2(v.z, SEL_HI, sm[5]); sm[6] = dot2(v.w, SEL_LO, sm[6]); sm[7] = dot2(v.w, SEL_HI, sm[7]);
      }
      u16* op = a.out + (size_t)((tbC * 4 + (rt ? tL : tE)) * 32 + tok) * DM + 64 * jj + 8 * c8;
      *(GAS v4u*)op = v4u{pk2(sm[0], sm[1]), pk2(sm[2], sm[3]), pk2(sm[4], sm[5]), pk2(sm[6], sm[7])};
    }
    ST(15);
  }
  asm volatile("hk_code_end:");
}
}

extern "C" void kernel_launch(void* const* d_in, const int* in_sizes, int n_in, void* d_out, int out_size, void* d_ws, size_t ws_size, hipStream_t stream) {
  using namespace hk;
  Args a;
  a.x = (const u16*)d_in[0]; a.wqkv = (const u16*)d_in[1]; a.wphi = (const u16*)d_in[2]; a.wo = (const u16*)d_in[3];
  a.wg = (const u16*)d_in[5]; a.wu = (const u16*)d_in[6]; a.wd = (const u16*)d_in[7];
  a.out = (u16*)d_out; a.ws = (char*)d_ws;
  (void)hipFuncSetAttribute((const void*)k_main, hipFuncAttributeMaxDynamicSharedMemorySize, LDS_BYTES);
  k_prep<<<(NFRAG_W + NFRAG_X) / 4, 256, 0, stream>>>(a);
  k_main<<<256, 512, LDS_BYTES, stream>>>(a);
}
```

```cpp
#include <hip/hip_runtime.h>

namespace hk {
typedef unsigned short u16;
typedef unsigned long long u64;
typedef unsigned v4u __attribute__((ext_vector_type(4)));
typedef unsigned v2u __attribute__((ext_vector_type(2)));
typedef float f32x16 __attribute__((ext_vector_type(16)));
typedef float f32x4 __attribute__((ext_vector_type(4)));
typedef float f32x2 __attribute__((ext_vector_type(2)));
typedef __bf16 bf16x8 __attribute__((ext_vector_type(8)));
typedef __bf16 bf16x2 __attribute__((ext_vector_type(2)));
typedef short s16x4 __attribute__((ext_vector_type(4)));
typedef __amdgpu_buffer_rsrc_t rsrc_t;
#define GAS __attribute__((address_space(1)))
#define LAS __attribute__((address_space(3)))
#define DI __device__ __forceinline__
#define __uint_as_float(u) __builtin_bit_cast(float, (unsigned)(u))
#define __float_as_uint(f) __builtin_bit_cast(unsigned, (float)(f))
#define __any(p) (__builtin_amdgcn_ballot_w64(p) != 0ull)
#define SCHED() __builtin_amdgcn_sched_barrier(0)
#define CFENCE() asm volatile("" ::: "memory")
#define VMW(n) asm volatile("s_waitcnt vmcnt(%0)" ::"i"(n) : "memory")

#define RSLP 2
#define FSLP 4
constexpr int SEQ = 2048, DM = 448, DFF = 1792;
constexpr int OFF_EPOCH = 0, OFF_F1 = 4096, OFF_FA = 8192  , OFF_F3 = 12288  ;
constexpr int OFF_WA = 65536;
constexpr int OFF_WD = OFF_WA + 16 * 2 * 28 * 1024;
constexpr int OFF_KVP = OFF_WD + 14 * 112 * 1024;
constexpr int OFF_O = OFF_KVP + 16 * 16 * 4096;
constexpr int OFF_H = OFF_O + 64 * 16 * 2048;
constexpr int OFF_ACT = OFF_H + 64 * 28 * 1024;
constexpr int OFF_HS = OFF_ACT + 64 * 112 * 1024;
constexpr int OFF_END = OFF_HS + 64 * 1024;
constexpr unsigned SENT = 0xFFFFFFFFu;
constexpr unsigned SPIN_CAP = 1u << 15, LSPIN_CAP = 1u << 21;
constexpr int AUX_SC1 = 16;

constexpr int L_X = 0;
constexpr int L_WK = 114688;
constexpr int L_RS = 143360;
constexpr int L_WPHI = 159744;
constexpr int L_SY = 161600;
constexpr int L_COR = 161792;
constexpr int L_JUNK = L_WPHI + 1600;
constexpr int L_WV = 0, L_WQ = 28672;
constexpr int L_PHI = L_RS;
constexpr int L_V = 4 * 16384;
constexpr int L_KV = 5 * 16384;
constexpr int L_OIMG = 57344;
constexpr int L_BK = 90112;
constexpr int L_BV = 0;
constexpr int L_ST2 = 147456;
constexpr int L_H = 0;
constexpr int L_WDL = 114688;
constexpr int L_RED = 0;
constexpr int LDS_BYTES = 163840;
constexpr int SY_VP = L_SY, SY_KP = L_SY + 16, SY_SK = L_SY + 32, SY_SV = L_SY + 48, SY_H = L_SY + 64  , SY_C1 = L_SY + 80, SY_C2 = L_SY + 84, SY_C3 = L_SY + 88, SY_S = L_SY + 92;

DI unsigned pk2(float a, float b) { f32x2 v = {a, b}; return __builtin_bit_cast(unsigned, __builtin_convertvector(v, bf16x2)); }
DI float lo_f(unsigned u) { return __uint_as_float(u << 16); }
DI float hi_f(unsigned u) { return __uint_as_float(u & 0xffff0000u); }
DI float dot2(unsigned a, unsigned b, float c) { return __builtin_amdgcn_fdot2_f32_bf16(__builtin_bit_cast(bf16x2, a), __builtin_bit_cast(bf16x2, b), c, false); }
DI f32x16 mfma32(v4u a, v4u b, f32x16 c) { return __builtin_amdgcn_mfma_f32_32x32x16_bf16(__builtin_bit_cast(bf16x8, a), __builtin_bit_cast(bf16x8, b), c, 0, 0, 0); }
DI f32x4 mfma16(v4u a, v4u b, f32x4 c) { return __builtin_amdgcn_mfma_f32_16x16x32_bf16(__builtin_bit_cast(bf16x8, a), __builtin_bit_cast(bf16x8, b), c, 0, 0, 0); }
DI void bar() { asm volatile("s_waitcnt lgkmcnt(0)" ::: "memory"); __builtin_amdgcn_s_barrier(); CFENCE(); }
DI float xsum(float v) { unsigned u = __float_as_uint(v); auto r = __builtin_amdgcn_permlane32_swap(u, u, false, false); return __uint_as_float(r[0]) + __uint_as_float(r[1]); }
DI float xmax(float v) { unsigned u = __float_as_uint(v); auto r = __builtin_amdgcn_permlane32_swap(u, u, false, false); return fmaxf(__uint_as_float(r[0]), __uint_as_float(r[1])); }
DI float xmin(float v) { unsigned u = __float_as_uint(v); auto r = __builtin_amdgcn_permlane32_swap(u, u, false, false); return fminf(__uint_as_float(r[0]), __uint_as_float(r[1])); }
DI float upper(float v) { unsigned u = __float_as_uint(v); auto r = __builtin_amdgcn_permlane32_swap(u, u, false, false); return __uint_as_float(r[1]); }
DI unsigned guard(unsigned u) { return u < 0xFFFFFFFEu ? u : 0xFFFFFFFEu; }
DI v4u guard4(v4u v) { return v4u{guard(v.x), guard(v.y), guard(v.z), guard(v.w)}; }
DI unsigned mx4(unsigned m, v4u a) { m = max(m, max(a.x, a.y)); return max(m, max(a.z, a.w)); }
DI v4u ldc(rsrc_t rs, int voff, int soff) { return __builtin_amdgcn_raw_buffer_load_b128(rs, voff, soff, AUX_SC1); }
DI void stc(rsrc_t rs, int voff, int soff, v4u v) { __builtin_amdgcn_raw_buffer_store_b128(v, rs, voff + soff, 0, AUX_SC1); }
DI v4u lds128(LAS char* L, int off) { return *(LAS v4u*)(L + off); }
DI v2u ldtr(LAS char* L, int off) { return __builtin_bit_cast(v2u, __builtin_amdgcn_ds_read_tr16_b64_v4i16((LAS s16x4*)(L + off))); }
DI v4u pack8(const f32x16& a, int s) { return v4u{pk2(a[8 * s], a[8 * s + 1]), pk2(a[8 * s + 2], a[8 * s + 3]), pk2(a[8 * s + 4], a[8 * s + 5]), pk2(a[8 * s + 6], a[8 * s + 7])}; }
DI void dma(int lds, int voff, rsrc_t r, int soff) {
  asm volatile("s_mov_b32 m0, %0\n\ts_nop 0\n\tbuffer_load_dwordx4 %1, %2, %3 offen lds" ::"s"(lds), "v"(voff), "s"(r), "s"(soff) : "memory");
}
DI void dma_c(int lds, int voff, rsrc_t r, int soff) {
  asm volatile("s_mov_b32 m0, %0\n\ts_nop 0\n\tbuffer_load_dwordx4 %1, %2, %3 offen sc1 lds" ::"s"(lds), "v"(voff), "s"(r), "s"(soff) : "memory");
}
DI void touch(int lds, int voff, rsrc_t r, int soff) {
  asm volatile("s_mov_b32 m0, %0\n\ts_nop 0\n\tbuffer_load_dword %1, %2, %3 offen lds" ::"s"(lds), "v"(voff), "s"(r), "s"(soff) : "memory");
}
DI void dma_nt(int lds, int voff, rsrc_t r, int soff) {
  asm volatile("s_mov_b32 m0, %0\n\ts_nop 0\n\tbuffer_load_dwordx4 %1, %2, %3 offen nt lds" ::"s"(lds), "v"(voff), "s"(r), "s"(soff) : "memory");
}
DI v2u ldc2(rsrc_t rs, int voff, int soff) { return __builtin_amdgcn_raw_buffer_load_b64(rs, voff, soff, AUX_SC1); }
DI void stflag(rsrc_t rs, int off, unsigned tlo, unsigned thi) { __builtin_amdgcn_raw_buffer_store_b64(v2u{tlo, thi}, rs, off, 0, AUX_SC1); }
DI void flag_ensure(v2u f, rsrc_t rs, int voff, int soff, unsigned tlo, unsigned thi) {
  if (__builtin_expect(__any(((f.x ^ tlo) | (f.y ^ thi)) != 0), 0)) {
#pragma unroll 1
    for (unsigned sp = 0; sp < SPIN_CAP; ++sp) {
      __builtin_amdgcn_s_sleep(FSLP);
      CFENCE();
      f = ldc2(rs, voff, soff);
      if (!__any(((f.x ^ tlo) | (f.y ^ thi)) != 0)) break;
    }
  }
  CFENCE();
}
DI void sig(LAS char* L, int off, unsigned v, int) {
  asm volatile("s_mov_b64 exec, 1\n\tds_write_b32 %0, %1\n\ts_mov_b64 exec, -1" ::"v"((unsigned)(size_t)(L + off)), "v"(v) : "memory");
}
DI void arrive(LAS char* L, int off, int) {
  asm volatile("s_mov_b64 exec, 1\n\tds_add_u32 %0, %1\n\ts_mov_b64 exec, -1" ::"v"((unsigned)(size_t)(L + off)), "v"(1u) : "memory");
}
#define PIN(x) asm volatile("" : "+v"(x))
DI unsigned min4u(v4u v) { return (unsigned)__builtin_amdgcn_readfirstlane(min(min(v.x, v.y), min(v.z, v.w))); }
DI void wait4(LAS char* L, int off, unsigned target) {
#pragma unroll 1
  for (unsigned sp = 0; sp < LSPIN_CAP; ++sp) {
    v4u v = *(volatile LAS v4u*)(L + off);
    if ((unsigned)__builtin_amdgcn_readfirstlane(min(min(v.x, v.y), min(v.z, v.w))) >= target) break;
    __builtin_amdgcn_s_sleep(1);
  }
  CFENCE();
}
DI void wait8(LAS char* L, unsigned vt, unsigned kt) {
#pragma unroll 1
  for (unsigned sp = 0; sp < LSPIN_CAP; ++sp) {
    v4u v = *(volatile LAS v4u*)(L + SY_VP), k = *(volatile LAS v4u*)(L + SY_KP);
    if (min4u(v) >= vt && min4u(k) >= kt) break;
    __builtin_amdgcn_s_sleep(1);
  }
  CFENCE();
}
DI void wait1(LAS char* L, int off, unsigned target) {
#pragma unroll 1
  for (unsigned sp = 0; sp < LSPIN_CAP; ++sp) {
    unsigned v = *(volatile LAS unsigned*)(L + off);
    if ((unsigned)__builtin_amdgcn_readfirstlane(v) >= target) break;
    __builtin_amdgcn_s_sleep(1);
  }
  CFENCE();
}

template <int V> struct IC { static constexpr int v = V; };
template <int I, int N, class F> DI void sfor(F&& f) { if constexpr (I < N) { f(IC<I>{}); sfor<I + 1, N>(f); } }
template <int NK, int P, bool REG_IS_A, class H> DI f32x16 gemm_hook(const v4u (&reg)[NK], LAS char* L, int base, f32x16 acc, H&& hook) {
  v4u buf[P];
#pragma unroll
  for (int i = 0; i < P; ++i) buf[i] = lds128(L, base + i * 1024);
  SCHED();
  sfor<0, NK>([&](auto I) __attribute__((always_inline)) {
    constexpr int ks = decltype(I)::v;
    acc = REG_IS_A ? mfma32(reg[ks], buf[ks % P], acc) : mfma32(buf[ks % P], reg[ks], acc);
    if (ks + P < NK) buf[ks % P] = lds128(L, base + (ks + P) * 1024);
    hook(I);
    SCHED();
  });
  PIN(acc);
  return acc;
}
template <int NK, int P, bool REG_IS_A> DI f32x16 gemm_lds(const v4u (&reg)[NK], LAS char* L, int base, f32x16 acc) {
  v4u buf[P];
#pragma unroll
  for (int i = 0; i < P; ++i) buf[i] = lds128(L, base + i * 1024);
  SCHED();
#pragma unroll
  for (int ks = 0; ks < NK; ++ks) {
    acc = REG_IS_A ? mfma32(reg[ks], buf[ks % P], acc) : mfma32(buf[ks % P], reg[ks], acc);
    if (ks + P < NK) buf[ks % P] = lds128(L, base + (ks + P) * 1024);
    SCHED();
  }
  PIN(acc);
  return acc;
}
template <int NK, int P> DI f32x16 gemm_cs(const v4u (&reg)[NK], LAS char* L, int base, f32x4& c4, unsigned ONES) {
  v4u buf[P];
  f32x16 acc = {};
#pragma unroll
  for (int i = 0; i < P; ++i) buf[i] = lds128(L, base + i * 1024);
  SCHED();
#pragma unroll
  for (int ks = 0; ks < NK; ks += 2) {
    const v4u b0 = buf[ks % P], b1 = buf[(ks + 1) % P];
    c4.x = dot2(b0.x, ONES, c4.x); c4.y = dot2(b0.y, ONES, c4.y); c4.z = dot2(b0.z, ONES, c4.z); c4.w = dot2(b0.w, ONES, c4.w);
    c4.x = dot2(b1.x, ONES, c4.x); c4.y = dot2(b1.y, ONES, c4.y); c4.z = dot2(b1.z, ONES, c4.z); c4.w = dot2(b1.w, ONES, c4.w);
    SCHED();
    acc = mfma32(b0, reg[ks], acc);
    acc = mfma32(b1, reg[ks + 1], acc);
    if (ks + P < NK) { buf[ks % P] = lds128(L, base + (ks + P) * 1024); buf[(ks + 1) % P] = lds128(L, base + (ks + 1 + P) * 1024); }
    SCHED();
  }
  PIN(acc);
  return acc;
}
template <int LO, int HI, int STRIDE, int N> DI void issue(v4u (&a)[N], rsrc_t rs, int voff, int soff) {
#pragma unroll
  for (int i = LO; i < HI; ++i) a[i] = ldc(rs, voff, soff + i * STRIDE);
}
template <int LO, int HI, int STRIDE, int N> DI void ensure(v4u (&a)[N], rsrc_t rs, int voff, int soff) {
  unsigned m = 0;
#pragma unroll
  for (int i = LO; i < HI; ++i) m = mx4(m, a[i]);
  if (__builtin_expect(__any(m == SENT), 0)) {
#pragma unroll 1
    for (unsigned sp = 0; sp < SPIN_CAP; ++sp) {
      __builtin_amdgcn_s_sleep(RSLP);
      CFENCE();
      issue<LO, HI, STRIDE>(a, rs, voff, soff);
      m = 0;
#pragma unroll
      for (int i = LO; i < HI; ++i) m = mx4(m, a[i]);
      if (!__any(m == SENT)) break;
    }
  }
}
DI void flags_wait(rsrc_t rs, int off, int lane, unsigned tlo, unsigned thi) {
#pragma unroll 1
  for (unsigned sp = 0; sp < SPIN_CAP; ++sp) {
    CFENCE();
    v4u a = ldc(rs, lane * 16, off), b = ldc(rs, lane * 16, off + 1024);
    unsigned d = (a.x ^ tlo) | (a.z ^ tlo) | (b.x ^ tlo) | (b.z ^ tlo) | (a.y ^ thi) | (a.w ^ thi) | (b.y ^ thi) | (b.w ^ thi);
    if (!__any(d != 0)) break;
  }
  CFENCE();
}
DI void delay(int n) { for (int i = 0; i < n; ++i) __builtin_amdgcn_s_sleep(1); }

#define ST(k)

struct Args { const u16 *x, *wqkv, *wphi, *wo, *wg, *wu, *wd; u16* out; char* ws; };

constexpr float L2E = 1.4426950408889634f;
#define SUM4(a, v) do { a.x = dot2(v.x, ONES, a.x); a.y = dot2(v.y, ONES, a.y); a.z = dot2(v.z, ONES, a.z); a.w = dot2(v.w, ONES, a.w); } while (0)
#define SQ4(a, v) do { a.x = dot2(v.x, v.x, a.x); a.y = dot2(v.y, v.y, a.y); a.z = dot2(v.z, v.z, a.z); a.w = dot2(v.w, v.w, a.w); } while (0)
#define HS4(a) ((a.x + a.y) + (a.z + a.w))
#define ACC8(sm, v) do { sm[0] = dot2(v.x, SEL_LO, sm[0]); sm[1] = dot2(v.x, SEL_HI, sm[1]); sm[2] = dot2(v.y, SEL_LO, sm[2]); sm[3] = dot2(v.y, SEL_HI, sm[3]); \
    sm[4] = dot2(v.z, SEL_LO, sm[4]); sm[5] = dot2(v.z, SEL_HI, sm[5]); sm[6] = dot2(v.w, SEL_LO, sm[6]); sm[7] = dot2(v.w, SEL_HI, sm[7]); } while (0)
DI unsigned hilo(float v) { unsigned h = pk2(v, 0.f) & 0xffffu; float r = v - lo_f(h); return h | (pk2(r, 0.f) << 16); }

template <class H> DI void feature_map(const f32x16& acc, float rstd, const v4u (&wp)[2], bool hh, f32x16& ep, f32x16& en, H&& hook) {
  f32x16 t = acc * rstd;
  v4u t0 = pack8(t, 0), t1 = pack8(t, 1);
  if (hh) { t1.z = 0; t1.w = 0; }
  f32x16 p = {};
  p = mfma32(wp[0], t0, p);
  p = mfma32(wp[1], t1, p);
  hook(IC<0>{}); SCHED();
  float mx = fmaxf(fmaxf(p[12], p[13]), fmaxf(p[14], p[15])), mn = fminf(fminf(p[12], p[13]), fminf(p[14], p[15]));
  mx = hh ? -__builtin_inff() : mx; mn = hh ? __builtin_inff() : mn;
#pragma unroll
  for (int i = 0; i < 12; i += 2) { mx = fmaxf(fmaxf(mx, p[i]), p[i + 1]); mn = fminf(fminf(mn, p[i]), p[i + 1]); }
  mx = xmax(mx); mn = xmin(mn);
  hook(IC<1>{}); SCHED();
  float bp = -mx * L2E, bn = mn * L2E;
  float bpp = hh ? -__builtin_inff() : bp, bnp = hh ? -__builtin_inff() : bn;
  float sp0 = 0.f, sp1 = 0.f, sn0 = 0.f, sn1 = 0.f;
  sfor<0, 4>([&](auto Q) __attribute__((always_inline)) {
    constexpr int q = decltype(Q)::v;
#pragma unroll
    for (int i = 4 * q; i < 4 * q + 4; ++i) {
      ep[i] = __builtin_amdgcn_exp2f(__builtin_fmaf(p[i], L2E, i < 12 ? bp : bpp));
      en[i] = __builtin_amdgcn_exp2f(__builtin_fmaf(p[i], -L2E, i < 12 ? bn : bnp));
    }
    if (q > 0) {
      sp0 += ep[4 * q - 4] + ep[4 * q - 3]; sp1 += ep[4 * q - 2] + ep[4 * q - 1]; sn0 += en[4 * q - 4] + en[4 * q - 3]; sn1 += en[4 * q - 2] + en[4 * q - 1];
    }
    hook(IC<2 + q>{}); SCHED();
  });
  sp0 += ep[12] + ep[13]; sp1 += ep[14] + ep[15]; sn0 += en[12] + en[13]; sn1 += en[14] + en[15];
  float rp = __builtin_amdgcn_rcpf(xsum(sp0 + sp1)), rn = __builtin_amdgcn_rcpf(xsum(sn0 + sn1));
  hook(IC<6>{}); SCHED();
  ep *= rp;
  hook(IC<7>{}); SCHED();
  en *= rn;
}

DI void retile_out(LAS char* L, rsrc_t rs, int l16, int a0, int au, int aj0, int aj1, int d0, int dj0, int dj1, bool mlo, bool mhi) {
  v2u lo[4], hi[4];
#pragma unroll
  for (int j = 0; j < 4; ++j) { lo[j] = ldtr(L, a0 + (j & 1) * aj0 + (j >> 1) * aj1); hi[j] = ldtr(L, a0 + au + (j & 1) * aj0 + (j >> 1) * aj1); }
#pragma unroll
  for (int j = 0; j < 4; ++j) {
    v4u f = {mlo ? 0u : lo[j].x, mlo ? 0u : lo[j].y, mhi ? 0u : hi[j].x, mhi ? 0u : hi[j].y};
    stc(rs, l16, d0 + (j & 1) * dj0 + (j >> 1) * dj1, f);
  }
}

#define KDLY 0
#define ODLY 6
#define HDLY 6
#define CPRIO 1
#define KE 0
#if defined(__HIP_DEVICE_COMPILE__)
#define NOPK __attribute__((target("no-packed-fp32-ops")))
#else
#define NOPK
#endif
#define AI __attribute__((always_inline))
__global__ void __launch_bounds__(512, 2) NOPK k_main(Args a) {
  extern __shared__ __attribute__((aligned(16))) char smem[];
  LAS char* L = (LAS char*)smem;
  const int lb = (int)(unsigned)(size_t)L;
  const int b = blockIdx.x, xcd = b & 7, ci = b >> 3;
  const int tid = threadIdx.x, lane = tid & 63, w = __builtin_amdgcn_readfirstlane(tid >> 6);
  ST(1);
  if (w == 7) {
    u64 pc; unsigned len;
    asm volatile("s_getpc_b64 %0\nhk_code_begin:\n\ts_mov_b32 %1, hk_code_end-hk_code_begin" : "=s"(pc), "=s"(len));
    touch(lb + L_JUNK, ((ci & 7) * 64 + lane) * 128, __builtin_amdgcn_make_buffer_rsrc((void*)pc, 0, len, 0x00020000), 0);
  }
  if (w == 3 && lane < 48) *(LAS unsigned*)(L + L_SY + lane * 4) = 0u;
  bar();
  const int r32 = lane & 31; const bool hh = lane >= 32; const int h1 = lane >> 5;
  const int q4 = (lane & 15) >> 2, p4 = lane & 3, blk = (lane >> 4) & 1, g4 = lane >> 4, c16 = lane & 15;
  const rsrc_t rs = __builtin_amdgcn_make_buffer_rsrc(a.ws, 0, OFF_END, 0x00020000);
  const rsrc_t rqkv = __builtin_amdgcn_make_buffer_rsrc((void*)a.wqkv, 0, DM * 3 * DM * 2, 0x00020000);
  const int l16 = lane * 16;
  unsigned ONES, SEL_LO, SEL_HI;
  asm volatile("s_mov_b32 %0, 0x3f803f80\n\ts_mov_b32 %1, 0x3f80\n\ts_mov_b32 %2, 0x3f800000" : "=s"(ONES), "=s"(SEL_LO), "=s"(SEL_HI));
  const int tbA = 4 * (xcd >> 1) + (ci >> 3), hA = 8 * (xcd & 1) + (ci & 7);
  const int t4 = w & 3, G = w >> 2;
  const int ttB = 8 * xcd + (ci >> 2), cgB = ci & 3;
  const int ntB = 8 * cgB + w;
  const bool actB = ntB < 28;
  const bool actC = ci < 28;
  const int cc = actC ? ci : 27;
  const int rC = cc / 7, tbC = 4 * (xcd >> 1) + rC, jj = cc % 7, eC = xcd & 1;
  const int TC0 = 8 * (7 * eC + jj), TC = TC0 + w;
  const int ksh = hA == 15 ? 8 : 0;
  const v4u sent4 = {SENT, SENT, SENT, SENT};
  const u64 tag0 = *(const __attribute__((address_space(4))) u64*)(a.ws + OFF_EPOCH) + 1ull;
  unsigned tlo = (unsigned)tag0, thi = (unsigned)(tag0 >> 32);
#define PIN_TAG() asm volatile("" : "+s"(tlo), "+s"(thi))

  v4u wc[28], wo[16], wor[14], wp[2], hs[7], csf;
  v2u xr[2];
  f32x4 c4c = {};
  const rsrc_t rwc = __builtin_amdgcn_make_buffer_rsrc((void*)(t4 < 2 ? a.wg : a.wu), 0, actC ? DM * DFF * 2 : 0, 0x00020000);
  const int wcv = (lane >> 3) * 3584 + (((((lane & 7) >> 1) ^ (((lane >> 4) & 1) | (t4 & 2))) << 1) | (lane & 1)) * 16;
  const int wcs = (t4 & 1) * 56 * 3584 + TC0 * 32 + G * 128;
#define WC1(i) wc[i] = __builtin_amdgcn_raw_buffer_load_b128(rwc, wcv, wcs + (((((i) / 7) + rC) & 3) * 112 + ((i) % 7) * 8) * 3584, 0)
  const rsrc_t rwo = __builtin_amdgcn_make_buffer_rsrc((void*)a.wo, 0, actB ? DM * DM * 2 : 0, 0x00020000);
  const int wov = (lane >> 3) * 896 + (((((lane & 7) >> 1) ^ ((lane >> 4) & 3)) << 1) | (lane & 1)) * 16;
  const int wos = t4 * 56 * 896 + cgB * 256 + G * 128;
#define WO1(i) wor[i] = __builtin_amdgcn_raw_buffer_load_b128(rwo, wov, wos + (((i) / 7) * 224 + ((i) % 7) * 8) * 896, 0)
#define WC_B(LO, HI) do { _Pragma("unroll") for (int i = LO; i < HI; ++i) WC1(i); SCHED(); } while (0)
#define WO_B(LO, HI) do { _Pragma("unroll") for (int i = LO; i < HI; ++i) WO1(i); SCHED(); } while (0)
#define XR_LOAD() do { const u16* xp = a.x + (size_t)(32 * ttB + c16) * DM + 16 * (actB ? ntB : 0) + 4 * g4; xr[0] = *(const GAS v2u*)xp; xr[1] = *(const GAS v2u*)(xp + 16 * DM); SCHED(); } while (0)
  const int sy_s = (G ? SY_SV : SY_SK);
  auto shuf_wo = [&](int buf, unsigned n) AI {
    wait4(L, sy_s, 2 * n + 1);
#pragma unroll
    for (int i = 0; i < 14; ++i) *(LAS v4u*)(L + buf + (i / 7) * 28672 + (7 * t4 + i % 7) * 1024 + l16) = wor[i];
    sig(L, sy_s + t4 * 4, 2 * n + 2, lane);
    const int r0 = 16 * (g4 >> 1) + 4 * (g4 & 1) + q4;
    const int ae = buf + r0 * 128 + ((t4 ^ ((r0 >> 1) & 3)) * 32) + p4 * 8, ao_ = buf + r0 * 128 + ((t4 ^ (((r0 >> 1) + 2) & 3)) * 32) + p4 * 8;
    wait4(L, sy_s, 2 * n + 2);
#pragma unroll
    for (int hd = 0; hd < 16; ++hd) {
      const int base = ((hd & 1) ? ao_ : ae) + hd * 28 * 128;
      v2u lo = ldtr(L, base), hi = ldtr(L, base + 1024);
      if (g4 == 3) hi = v2u{0, 0};
      wo[hd] = v4u{lo.x, lo.y, hi.x, hi.y};
    }
    sig(L, sy_s + t4 * 4, 2 * n + 3, lane);
#pragma unroll
    for (int hd = 0; hd < 16; ++hd) PIN(wo[hd]);
  };
  auto shuf_wc = [&](auto HALF, int buf, unsigned n) AI {
    constexpr int o = 14 * decltype(HALF)::v;
    wait4(L, sy_s, 2 * n + 1);
#pragma unroll
    for (int i = 0; i < 14; ++i) *(LAS v4u*)(L + buf + (i / 7) * 28672 + (7 * t4 + i % 7) * 1024 + l16) = wc[o + i];
    sig(L, sy_s + t4 * 4, 2 * n + 2, lane);
    const int ad = buf + blk * 14336 + (4 * h1 + q4) * 128 + ((t4 ^ ((q4 >> 1) | (blk << 1))) * 32) + p4 * 8;
    wait4(L, sy_s, 2 * n + 2);
#pragma unroll
    for (int i = 0; i < 14; ++i) {
      v2u lo = ldtr(L, ad + (i / 7) * 28672 + (i % 7) * 2048), hi = ldtr(L, ad + (i / 7) * 28672 + (i % 7) * 2048 + 1024);
      wc[o + i] = v4u{lo.x, lo.y, hi.x, hi.y};
    }
    sig(L, sy_s + t4 * 4, 2 * n + 3, lane);
#pragma unroll
    for (int i = 0; i < 14; ++i) { SUM4(c4c, wc[o + i]); PIN(wc[o + i]); }
    PIN(c4c);
  };
  auto load_wp = [&]() AI {
#pragma unroll
    for (int s = 0; s < 2; ++s) {
      v2u lo = ldtr(L, L_WPHI + (16 * s + 4 * h1 + q4) * 56 + blk * 32 + p4 * 8);
      v2u hi = ldtr(L, L_WPHI + (16 * s + 8 + 4 * h1 + q4) * 56 + blk * 32 + p4 * 8);
      if (s == 1 && hh) hi = v2u{0, 0};
      if (r32 >= 28) { lo = v2u{0, 0}; hi = v2u{0, 0}; }
      wp[s] = v4u{lo.x, lo.y, hi.x, hi.y};
    }
    PIN(wp[0]); PIN(wp[1]);
  };
  auto publish_flag = [&](int cnt_off, int foff, unsigned last) AI {
    CFENCE();
    if (lane == 0) {
      unsigned old = __hip_atomic_fetch_add((LAS unsigned*)(L + cnt_off), 1u, __ATOMIC_RELAXED, __HIP_MEMORY_SCOPE_WORKGROUP);
      if (old == last) stflag(rs, foff, tlo, thi);
    }
    CFENCE();
  };
  const int slotB = (g4 & 1) * 32 + (hh ? 16 : 0) + c16;
  auto phase_b2 = [&](auto hook) AI {
    wait4(L, SY_KP, 11);
    ST(12);
    const int bo = L_OIMG + (g4 >> 1) * 1024 + ((g4 & 1) * 32 + c16) * 16;
    f32x4 h0 = {}, hq = {};
    v4u ob[8];
#pragma unroll
    for (int i = 0; i < 8; ++i) ob[i] = lds128(L, bo + (i >> 1) * 2048 + (i & 1) * 256);
    SCHED();
    sfor<0, 32>([&](auto I) AI {
      constexpr int i = decltype(I)::v;
      if (i & 1) hq = mfma16(wo[i >> 1], ob[i % 8], hq); else h0 = mfma16(wo[i >> 1], ob[i % 8], h0);
      if (i + 8 < 32) ob[i % 8] = lds128(L, bo + ((i + 8) >> 1) * 2048 + (i & 1) * 256);
      hook(I);
      SCHED();
    });
    unsigned a0 = pk2(h0.x + lo_f(xr[0].x), h0.y + hi_f(xr[0].x)), a1 = pk2(h0.z + lo_f(xr[0].y), h0.w + hi_f(xr[0].y));
    unsigned b0 = pk2(hq.x + lo_f(xr[1].x), hq.y + hi_f(xr[1].x)), b1 = pk2(hq.z + lo_f(xr[1].y), hq.w + hi_f(xr[1].y));
    auto s0 = __builtin_amdgcn_permlane32_swap(a0, b0, false, false);
    auto s1 = __builtin_amdgcn_permlane32_swap(a1, b1, false, false);
    const v4u hv = guard4(v4u{s0[0], s1[0], s0[1], s1[1]});
    stc(rs, slotB * 16, actB ? OFF_H + (ttB * 28 + ntB) * 1024 : OFF_END, hv);
    SCHED();
    f32x4 a1_ = {}, a2_ = {};
    SUM4(a1_, hv); SQ4(a2_, hv);
    *(LAS f32x2*)(L + L_ST2 + (slotB & 31) * 144 + (slotB >> 5) * 64 + w * 8) = f32x2{HS4(a1_), HS4(a2_)};
    arrive(L, SY_S, lane);
    ST(13);
  };
  const int hso = OFF_H + tbC * 114688 + G * 28672 + ((t4 + rC) & 3) * 7168;
  const int hlo = L_H + G * 28672 + t4 * 7168 + l16;
  auto h_stage = [&](int round) AI {
    ensure<0, 7, 1024>(hs, rs, l16, hso + round * 57344);
#pragma unroll
    for (int i = 0; i < 7; ++i) *(LAS v4u*)(L + hlo + round * 57344 + i * 1024) = hs[i];
    arrive(L, SY_H + (G + 2 * round) * 4, lane);
  };

  if (w < 4) {
    ST(25);
    const int lrs = L_RS + w * 4096;
    const int id1 = w * 256 + b;
    int a0, aj0, aj1, d0, dj0, dj1; bool mlo = false;
    if (id1 < 224) {
      const int ks = id1 >> 3, pi = (id1 >> 2) & 1, qd = id1 & 3;
      const int ch = c16 < 14 ? c16 : 13;
      const int vo = g4 * 2688 + ch * 16, so = 16 * ks * 2688 + (pi ? 0 : 1792) + 224 * qd;
#pragma unroll
      for (int i = 0; i < 4; ++i) dma(lb + lrs + i * 1024, vo, rqkv, so + i * 4 * 2688);
      a0 = lrs + (8 * h1 + q4) * 256 + blk * 32 + p4 * 8; aj0 = 56; aj1 = 112;
      d0 = OFF_WA + (8 * qd + pi) * 28672 + ks * 1024; dj0 = 57344; dj1 = 114688; mlo = r32 >= 28;
    } else {
      const int u = id1 - 224, T2 = u / 7, c64 = u % 7;
      const rsrc_t rd = __builtin_amdgcn_make_buffer_rsrc((void*)a.wd, 0, DM * DFF * 2, 0x00020000);
      const int vo = (lane >> 3) * 896 + (lane & 7) * 16;
      if (id1 < 616) {
#pragma unroll
        for (int i = 0; i < 4; ++i) dma(lb + lrs + i * 1024, vo, rd, (32 * T2 + 8 * i) * 896 + 128 * c64);
      }
      a0 = lrs + (4 * h1 + q4) * 128 + blk * 32 + p4 * 8; aj0 = 64; aj1 = 2048;
      d0 = id1 < 616 ? OFF_WD + (2 * c64 * 112 + 2 * T2) * 1024 : OFF_END; dj0 = 114688; dj1 = 1024;
    }
    ST(26);
    if (w >= 2) {
      const rsrc_t rp = __builtin_amdgcn_make_buffer_rsrc((void*)a.wphi, 0, 16 * 28 * 28 * 2, 0x00020000);
      if ((w - 2) * 64 + lane < 98) dma(lb + L_WPHI + (w - 2) * 1024, l16, rp, hA * 1568 + (w - 2) * 1024);
    }
    PIN_TAG();
    VMW(0);
    ST(2);
    if (id1 < 616) retile_out(L, rs, l16, a0, 1024, aj0, aj1, d0, dj0, dj1, mlo, mlo);
    if (id1 < 224) { VMW(0); if (lane == 0) stflag(rs, OFF_FA + (id1 & 3) * 512 + (id1 >> 2) * 8, tlo, thi); }
    const int pf0 = OFF_KVP + (hA * 16 + tbA) * 4096 + w * 1024, pf1 = OFF_O + ((tbA * 4 + w) * 16 + hA) * 2048;
    const int pf3 = OFF_H + (ttB * 28 + 8 * cgB + w) * 1024, pf4 = cgB < 3 ? pf3 + 4096 : OFF_END;
    const int pf5 = (w == 3 && lane < 16) ? OFF_HS + (ttB * 4 + cgB) * 256 : OFF_END;
    ST(3);
    f32x16 acc = {};
    f32x4 c4 = {};
    {
      const int row = 32 * w + r32, key = (row >> 1) & 7;
      int xa[4];
#pragma unroll
      for (int i = 0; i < 4; ++i) xa[i] = L_X + row * 128 + (((2 * i + h1) ^ key) * 16);
      const int wa = L_WK + (8 * h1 + q4) * 64 + blk * 32 + p4 * 8 + ksh;
      unsigned prog = 0;
      sfor<0, 7>([&](auto S) AI {
        constexpr int s = decltype(S)::v;
        if (prog <= (unsigned)s) wait4(L, SY_VP, s + 1);
        v4u xb[4]; v2u wl[4], wh[4];
#pragma unroll
        for (int i = 0; i < 4; ++i) { wl[i] = ldtr(L, wa + s * 4096 + i * 1024); wh[i] = ldtr(L, wa + s * 4096 + i * 1024 + 256); xb[i] = lds128(L, xa[i] + s * 16384); }
        SCHED();
#pragma unroll
        for (int i = 0; i < 4; ++i) { v4u wf = {wl[i].x, wl[i].y, wh[i].x, wh[i].y}; acc = mfma32(wf, xb[i], acc); }
        v4u pv = *(volatile LAS v4u*)(L + SY_VP);
        SCHED();
#pragma unroll
        for (int i = 0; i < 4; ++i) { v4u wf = {wl[i].x, wl[i].y, wh[i].x, wh[i].y}; SUM4(c4, wf); }
        PIN(acc); PIN(c4);
        prog = min4u(pv);
        sig(L, SY_KP + w * 4, s + 1, lane);
        if (s < 5) stc(rs, l16, s == 0 ? pf0 : s == 1 ? pf1 : s == 2 ? pf1 + 1024 : s == 3 ? pf3 : pf4, sent4);
        if (s == 0) stc(rs, l16, pf5, sent4);
        if (s == 4) { wait4(L, SY_KP, 1); load_wp(); }
      });
    }
    ST(4);
    __builtin_amdgcn_s_setprio(2);
    VMW(0);
    publish_flag(SY_C1, OFF_F1 + b * 8, 3);
    {
      unsigned hl = hilo(xsum(HS4(c4)));
      PIN(hl);
      v4u cr;
#pragma unroll 1
      for (unsigned sp = 0; sp < LSPIN_CAP; ++sp) {
        unsigned pv = *(volatile LAS unsigned*)(L + SY_VP + w * 4);
        cr = *(volatile LAS v4u*)(L + L_COR + w * 512 + r32 * 16);
        if ((unsigned)__builtin_amdgcn_readfirstlane(pv) >= 8u) break;
      }
      ST(29);
      v4u ca = {hh ? 0u : hl, hh ? 0u : hl, 0u, 0u}, cb = {hh ? 0u : cr.x, hh ? 0u : cr.y, 0u, 0u};
      acc = mfma32(ca, cb, acc);
      f32x16 ep, en;
      feature_map(acc, __uint_as_float(cr.z), wp, hh, ep, en, [&](auto I) AI { constexpr int i = decltype(I)::v; WO1(i); if (i >= 2) WO1(6 + i); });
      const int ro = L_PHI + (32 * w + r32) * 128, sw = r32 & 15;
#pragma unroll
      for (int g = 0; g < 4; ++g) {
        *(LAS v2u*)(L + ro + (((2 * g + h1) ^ sw) * 8)) = v2u{pk2(ep[4 * g], ep[4 * g + 1]), pk2(ep[4 * g + 2], ep[4 * g + 3])};
        *(LAS v2u*)(L + ro + (((8 + 2 * g + h1) ^ sw) * 8)) = v2u{pk2(en[4 * g], en[4 * g + 1]), pk2(en[4 * g + 2], en[4 * g + 3])};
      }
      sig(L, SY_KP + w * 4, 8, lane);
    }
    ST(5);
    v2u f1 = ldc2(rs, lane * 8, OFF_F1 + w * 512);
    WC_B(0, 4);
    {
      const int r0 = 8 * g4 + q4, r1 = r0 + 4;
      const int ta0 = L_PHI + r0 * 128 + (((4 * w + p4) ^ (r0 & 15)) * 8), ta1 = L_PHI + r1 * 128 + (((4 * w + p4) ^ (r1 & 15)) * 8);
      const int tv = L_V + r0 * 72 + p4 * 8;
      wait8(L, 10, 8);
      ST(6);
      v2u fa0[4], fa1[4], b00[4], b01[4], b10[4], b11[4];
#pragma unroll
      for (int s = 0; s < 4; ++s) {
        fa0[s] = ldtr(L, ta0 + s * 4096); fa1[s] = ldtr(L, ta1 + s * 4096);
        b00[s] = ldtr(L, tv + s * 4096); b01[s] = ldtr(L, tv + s * 4096 + 288);
        b10[s] = ldtr(L, tv + s * 4096 + 32); b11[s] = ldtr(L, tv + s * 4096 + 288 + 32);
      }
      SCHED();
      f32x4 z0 = {}, z1 = {};
#pragma unroll
      for (int s = 0; s < 4; ++s) {
        const v4u af_ = {fa0[s].x, fa0[s].y, fa1[s].x, fa1[s].y};
        z0 = mfma16(af_, v4u{b00[s].x, b00[s].y, b01[s].x, b01[s].y}, z0);
        z1 = mfma16(af_, v4u{b10[s].x, b10[s].y, b11[s].x, b11[s].y}, z1);
      }
      auto s0 = __builtin_amdgcn_permlane32_swap(pk2(z0.x, z0.y), pk2(z1.x, z1.y), false, false);
      auto s1 = __builtin_amdgcn_permlane32_swap(pk2(z0.z, z0.w), pk2(z1.z, z1.w), false, false);
      stc(rs, slotB * 16, OFF_KVP + (hA * 16 + tbA) * 4096 + w * 1024, guard4(v4u{s0[0], s1[0], s0[1], s1[1]}));
      SCHED();
    }
    ST(7);
    __builtin_amdgcn_s_setprio(1);
    flag_ensure(f1, rs, lane * 8, OFF_F1 + w * 512, tlo, thi);
    sig(L, SY_SK + w * 4, 1, lane);
    shuf_wo(L_BK, 0);
    delay(KDLY);
    {
      v4u kp[16];
      const int ko = OFF_KVP + hA * 16 * 4096 + w * 1024;
      int kso[4];
#pragma unroll
      for (int g = 0; g < 4; ++g) kso[g] = ko + ((g + tbA) & 3) * 16384 - g * 16384;
      issue<0, 4, 4096>(kp, rs, l16, kso[0]); issue<4, 8, 4096>(kp, rs, l16, kso[1]); issue<8, 12, 4096>(kp, rs, l16, kso[2]); issue<12, 16, 4096>(kp, rs, l16, kso[3]);
      SCHED();
      sig(L, SY_KP + w * 4, 9, lane);
      ST(8);
      float sm[8] = {0.f, 0.f, 0.f, 0.f, 0.f, 0.f, 0.f, 0.f};
#define KGRP(g) do { ensure<4 * g, 4 * g + 4, 4096>(kp, rs, l16, kso[g]); _Pragma("unroll") for (int i = 4 * g; i < 4 * g + 4; ++i) ACC8(sm, kp[i]); \
      _Pragma("unroll") for (int j = 0; j < 8; ++j) PIN(sm[j]); } while (0)
      KGRP(0); KGRP(1); KGRP(2); KGRP(3);
      *(LAS v4u*)(L + L_KV + w * 1024 + l16) = v4u{pk2(sm[0], sm[1]), pk2(sm[2], sm[3]), pk2(sm[4], sm[5]), pk2(sm[6], sm[7])};
      sig(L, SY_KP + w * 4, 10, lane);
    }
    __builtin_amdgcn_s_setprio(0);
    ST(9);
    XR_LOAD();
    WC_B(4, 10);
    wait4(L, SY_VP, 12);
    ST(10);
    WC_B(10, 14);
    delay(ODLY);
    __builtin_amdgcn_s_setprio(1);
    {
      v4u of[8];
      const int oo = OFF_O + ttB * 32768 + w * 8192;
      issue<0, 8, 1024>(of, rs, l16, oo);
      SCHED();
      ST(11);
      shuf_wc(IC<0>{}, L_BK, 1);
      ensure<0, 4, 1024>(of, rs, l16, oo);
#pragma unroll
      for (int i = 0; i < 4; ++i) *(LAS v4u*)(L + L_OIMG + w * 8192 + i * 1024 + l16) = of[i];
      ensure<4, 8, 1024>(of, rs, l16, oo);
#pragma unroll
      for (int i = 4; i < 8; ++i) *(LAS v4u*)(L + L_OIMG + w * 8192 + i * 1024 + l16) = of[i];
      sig(L, SY_KP + w * 4, 11, lane);
    }
    WC_B(14, 18);
    phase_b2([&](auto I) AI { constexpr int i = decltype(I)::v; if (i % 3 == 0 && 18 + i / 3 < 28) WC1(18 + i / 3); });
    if (w == 3) {
      wait1(L, SY_S, 8);
      const int so = L_ST2 + r32 * 144 + h1 * 64;
      f32x4 p0 = *(LAS f32x4*)(L + so), p1 = *(LAS f32x4*)(L + so + 16), p2 = *(LAS f32x4*)(L + so + 32), p3 = *(LAS f32x4*)(L + so + 48);
      p0 += p1;
      if (cgB < 3) p0 += p2 + p3;
      float s1 = xsum(p0.x + p0.z), s2 = xsum(p0.y + p0.w);
      __builtin_amdgcn_raw_buffer_store_b64(v2u{guard(__float_as_uint(s1)), guard(__float_as_uint(s2))}, rs, hh ? OFF_END : OFF_HS + (ttB * 4 + cgB) * 256 + r32 * 8, 0, AUX_SC1);
    } else delay(HDLY);
    if (b == 0 && tid == 0) *(GAS v2u*)(a.ws + OFF_EPOCH) = v2u{tlo, thi};
    if (actC) {
      issue<0, 7, 1024>(hs, rs, l16, hso);
      SCHED();
      shuf_wc(IC<1>{}, L_BK, 2);
      wait4(L, SY_SV, 5);
      h_stage(0);
    }
    __builtin_amdgcn_s_setprio(0);
  } else {
    const rsrc_t rx = __builtin_amdgcn_make_buffer_rsrc((void*)a.x, 0, SEQ * DM * 2, 0x00020000);
    ST(25);
    if (lane < 56) {
      if (t4 < 2) touch(lb + L_JUNK, (ci * 112 + t4 * 56 + lane) * 128, rx, (xcd >> 1) * 512 * 896);
      else if (t4 == 2) { const int j = ci * 56 + lane; touch(lb + L_JUNK, (j >> 2) * 2688 + (j & 3) * 128, rqkv, (7 + 3 * (xcd & 1)) * 128); }
    }
    const int xr_ = lane >> 3;
    const int ve = xr_ * 896 + (((lane & 7) ^ (xr_ >> 1)) * 16), vod = xr_ * 896 + (((lane & 7) ^ (4 + (xr_ >> 1))) * 16);
    const int vk = (lane >> 2) * 2688 + (lane & 3) * 16;
    const int sx = (tbA * 128 + 32 * t4) * 896, sk = 16 * t4 * 2688 + 896 + 56 * hA - ksh;
    auto seg_issue = [&](int s) AI {
      dma(lb + L_WK + (64 * s + 16 * t4) * 64, vk, rqkv, sk + 64 * s * 2688);
#pragma unroll
      for (int i = 0; i < 4; ++i) dma(lb + L_X + s * 16384 + (32 * t4 + 8 * i) * 128, (i & 1) ? vod : ve, rx, sx + 8 * i * 896 + s * 128);
    };
    seg_issue(0); ST(26); seg_issue(1);
    ST(27);
    PIN_TAG();
    v4u xf[28], phq[4];
    f32x4 a1 = {}, a2 = {};
    v2u fa;
    const int fao = OFF_FA + (hA >> 2) * 512, fav = (lane < 56 ? lane : 55) * 8;
    const int wao = OFF_WA + hA * 57344 + 7 * t4 * 1024;
    {
      const int row = 32 * t4 + r32, key = (row >> 1) & 7;
#pragma unroll
      for (int s = 0; s < 7; ++s) {
        VMW(s < 4 ? 5 : s == 4 ? 6 : s == 5 ? 5 : 7);
        sig(L, SY_VP + t4 * 4, s + 1, lane);
        if (s == 0) ST(3);
        if (s == 3) ST(28);
#pragma unroll
        for (int i = 0; i < 4; ++i) xf[4 * s + i] = lds128(L, L_X + s * 16384 + row * 128 + (((2 * i + h1) ^ key) * 16));
        if (s < 5) seg_issue(s + 2);
        if (s == 3) asm volatile("buffer_load_dwordx2 %0, %1, %2, %3 offen sc1" : "=v"(fa) : "v"(fav), "s"(rs), "s"(fao) : "memory");
        if (s == 5) {
          PIN(fa);
          flag_ensure(fa, rs, fav, fao, tlo, thi);
          wait8(L, 3, 2);
#pragma unroll
          for (int i = 0; i < 7; ++i) dma_c(lb + L_WV + (7 * t4 + i) * 1024, l16, rs, wao + i * 1024);
        }
        SCHED();
#pragma unroll
        for (int i = 0; i < 4; ++i) { SUM4(a1, xf[4 * s + i]); SQ4(a2, xf[4 * s + i]); }
        PIN(a1); PIN(a2);
        SCHED();
      }
    }
    ST(4);
    __builtin_amdgcn_s_setprio(3);
    const float mean = xsum(HS4(a1)) * (1.f / DM), var = fmaxf(xsum(HS4(a2)) * (1.f / DM) - mean * mean, 0.f);
    const float rstd = __builtin_amdgcn_rsqf(var + 1e-5f);
    const unsigned hl = hilo(-mean);
    const unsigned mh = (hl & 0xffffu) * 0x10001u, ml = (hl >> 16) * 0x10001u;
    if (!hh) *(LAS v4u*)(L + L_COR + t4 * 512 + r32 * 16) = v4u{mh, ml, __float_as_uint(rstd), 0u};
    const v4u mfr = {hh ? 0u : mh, hh ? 0u : ml, 0u, 0u};
    sig(L, SY_VP + t4 * 4, 8, lane);
    ST(5);
    auto cs_part = [&](int p, int base) AI {
      f32x4 c4 = {};
      v4u f[7];
#pragma unroll
      for (int i = 0; i < 7; ++i) f[i] = lds128(L, base + (7 * t4 + i) * 1024 + l16);
#pragma unroll
      for (int i = 0; i < 7; ++i) SUM4(c4, f[i]);
      *(LAS float*)(L + L_V + t4 * 4096 + 2304 + p * 256 + lane * 4) = HS4(c4);
    };
    float cp[4];
    auto cs_read = [&](int p) AI {
#pragma unroll
      for (int i = 0; i < 4; ++i) cp[i] = *(LAS float*)(L + L_V + i * 4096 + 2304 + p * 256 + lane * 4);
    };
    auto cs_frag = [&]() AI -> v4u {
      unsigned x = hilo(xsum((cp[0] + cp[1]) + (cp[2] + cp[3])));
      x = hh ? 0u : x;
      return v4u{x, x, 0u, 0u};
    };
    wait8(L, 5, 5);
    VMW(0);
    cs_part(0, L_WV);
    sig(L, SY_VP + t4 * 4, 9, lane);
    wait4(L, SY_VP, 9);
    ST(6);
    cs_read(0);
    v4u csv;
    f32x16 acc = gemm_hook<28, 6, false>(xf, L, L_WV + l16, f32x16{}, [&](auto I) AI {
      constexpr int ks = decltype(I)::v;
      if (ks % 3 == 0 && ks / 3 < 7) dma_c(lb + L_WQ + (7 * t4 + ks / 3) * 1024, l16, rs, wao + 28672 + (ks / 3) * 1024);
      if (ks == 8) { csv = cs_frag(); PIN(csv); }
    });
    acc = mfma32(csv, mfr, acc);
    acc *= rstd;
    if (hh) acc[12] = 1.0f;
    {
      const int ro = L_V + t4 * 4096 + r32 * 72 + h1 * 8;
#pragma unroll
      for (int g = 0; g < 4; ++g) *(LAS v2u*)(L + ro + g * 16) = v2u{pk2(acc[4 * g], acc[4 * g + 1]), pk2(acc[4 * g + 2], acc[4 * g + 3])};
    }
    sig(L, SY_VP + t4 * 4, 10, lane);
    __builtin_amdgcn_s_setprio(0);
    ST(7);
    load_wp();
    VMW(0);
    WO_B(0, 6);
    cs_part(1, L_WQ);
    sig(L, SY_VP + t4 * 4, 11, lane);
    wait4(L, SY_VP, 11);
    cs_read(1);
    acc = gemm_hook<28, 6, false>(xf, L, L_WQ + l16, f32x16{}, [&](auto I) AI {
      constexpr int ks = decltype(I)::v;
      if (ks % 2 == 0 && 6 + ks / 2 < 14) WO1(6 + ks / 2);
      if (ks == 8) { csv = cs_frag(); PIN(csv); }
    });
    sig(L, SY_SV + t4 * 4, 1, lane);
    acc = mfma32(csv, mfr, acc);
    {
      f32x16 ep, en;
      feature_map(acc, rstd, wp, hh, ep, en, [&](auto) AI {});
      phq[0] = pack8(ep, 0); phq[1] = pack8(ep, 1); phq[2] = pack8(en, 0); phq[3] = pack8(en, 1);
      PIN(phq[0]); PIN(phq[1]); PIN(phq[2]); PIN(phq[3]);
    }
    ST(8);
    wait4(L, SY_KP, 9);
    XR_LOAD();
    WC_B(0, 4);
    __builtin_amdgcn_s_setprio(3);
    f32x16 o = {};
    {
      v4u kf[4];
#pragma unroll 1
      for (unsigned sp = 0; sp < LSPIN_CAP; ++sp) {
        v4u pv = *(volatile LAS v4u*)(L + SY_KP);
#pragma unroll
        for (int s = 0; s < 4; ++s) kf[s] = *(volatile LAS v4u*)(L + L_KV + s * 1024 + l16);
        if (min4u(pv) >= 10u) break;
        __builtin_amdgcn_s_sleep(1);
      }
      ST(9);
#pragma unroll
      for (int s = 0; s < 4; ++s) o = mfma32(kf[s], phq[s], o);
    }
    const float rd = __builtin_amdgcn_rcpf(upper(o[12]) + 1e-6f);
    o *= rd;
    v4u o0 = pack8(o, 0), o1 = pack8(o, 1);
    if (hh) { o1.z = 0; o1.w = 0; }
    const int oo = OFF_O + ((tbA * 4 + t4) * 16 + hA) * 2048;
    stc(rs, l16, oo, guard4(o0)); stc(rs, l16, oo + 1024, guard4(o1));
    SCHED();
    __builtin_amdgcn_s_setprio(0);
    sig(L, SY_VP + t4 * 4, 12, lane);
    ST(10);
    shuf_wo(L_BV, 0);
    WC_B(4, 14);
    ST(11);
    phase_b2([&](auto I) AI { constexpr int i = decltype(I)::v; if (i % 4 == 0 && 14 + i / 4 < 21) WC1(14 + i / 4); });
    if (actC) {
      WC_B(21, 28);
      shuf_wc(IC<0>{}, L_BV, 1);
      issue<0, 7, 1024>(hs, rs, l16, hso);
      const int hsv = r32 * 8 + h1 * 512, hss = OFF_HS + (tbC * 4 + t4) * 1024;
      v2u q0 = ldc2(rs, hsv, hss), q1 = ldc2(rs, hsv, hss + 256);
      SCHED();
      wait4(L, SY_SK, 7);
      shuf_wc(IC<1>{}, L_BK, 2);
#pragma unroll 1
      for (unsigned sp = 0; sp < SPIN_CAP; ++sp) {
        if (__builtin_expect(!__any(max(max(q0.x, q0.y), max(q1.x, q1.y)) == SENT), 1)) break;
        __builtin_amdgcn_s_sleep(RSLP);
        CFENCE();
        q0 = ldc2(rs, hsv, hss); q1 = ldc2(rs, hsv, hss + 256);
      }
      float mean2 = xsum(__uint_as_float(q0.x) + __uint_as_float(q1.x)) * (1.f / DM);
      float var2 = fmaxf(xsum(__uint_as_float(q0.y) + __uint_as_float(q1.y)) * (1.f / DM) - mean2 * mean2, 0.f) + 1e-5f;
      unsigned hl2 = hilo(-mean2);
      if (!hh) *(LAS v4u*)(L + L_COR + t4 * 512 + r32 * 16) = v4u{(hl2 & 0xffffu) * 0x10001u, (hl2 >> 16) * 0x10001u, __float_as_uint(__builtin_amdgcn_rsqf(var2)), __float_as_uint(var2)};
      h_stage(0);
    }
  }
  if (actC) {
    ST(14);
    { unsigned hl = hilo(xsum(HS4(c4c))); hl = hh ? 0u : hl; csf = v4u{hl, hl, 0u, 0u}; PIN(csf); }
    const int ao = OFF_ACT + (tbC * 4 * 112 + TC) * 1024;
    wait1(L, SY_H, 4);
    ST(15);
    f32x16 accA, accB;
    accA = gemm_hook<28, 6, true>(wc, L, L_H + l16, f32x16{}, [&](auto I) AI {
      constexpr int ks = decltype(I)::v;
      if (ks % 3 == 1 && ks / 3 < 7) hs[ks / 3] = ldc(rs, l16, hso + 57344 + (ks / 3) * 1024);
      if (ks % 6 == 0 && ks / 6 < 4) stc(rs, l16, ao + (ks / 6) * 114688, sent4);
    });
    ST(16);
    v4u wd[28];
    int rot[7];
#pragma unroll
    for (int p = 0; p < 7; ++p) { int r_ = p + jj; rot[p] = (r_ >= 7 ? r_ - 7 : r_) * 2048 + 14 * w * 1024; }
    const int wdo = OFF_WD + 2 * jj * 112 * 1024;
#define WD_OFF(n) (wdo + rot[((n) % 14) >> 1] + ((n) & 1) * 1024 + ((n) >= 14 ? 114688 : 0))
    constexpr int NPRE = 11, NLDS = 4;
    wait1(L, SY_H + 4, 4);
    ST(17);
    const int tE = eC ? 1 : 0, tL = eC ? 2 : 3;
    const int rt = tid >> 8, tok = (tid >> 3) & 31, c8 = tid & 7;
    float k1, k2, vr, tz[8], tn[8]; unsigned ow[4];
    v4u cr = lds128(L, L_COR + r32 * 16), crn = lds128(L, L_COR + 512 + r32 * 16);
    v4u buf[4];
#pragma unroll
    for (int i = 0; i < 4; ++i) buf[i] = lds128(L, L_H + 28672 + i * 1024 + l16);
    accA = mfma32(csf, v4u{hh ? 0u : cr.x, hh ? 0u : cr.y, 0u, 0u}, accA);
    auto epi_consts = [&]() AI {
      const float rr = __uint_as_float(cr.z);
      vr = __uint_as_float(cr.w);
      k1 = (-2.f * 0.7978845608028654f * L2E) * rr; k2 = k1 * 0.044715f * rr * rr;
    };
    auto epi_step = [&](const f32x16& pv, int s) AI {
      const int e = s / 3, ph = s % 3;
      if (ph == 0) { float g = pv[e]; tz[e] = __builtin_fmaf(k2, g * g, k1) * g; PIN(tz[e]); }
      else if (ph == 1) { tz[e] = __builtin_amdgcn_exp2f(tz[e]); tn[e] = pv[e] * pv[e + 8]; tz[e] = __builtin_fmaf(tz[e], vr, vr); PIN(tz[e]); PIN(tn[e]); }
      else { tn[e] *= __builtin_amdgcn_rcpf(tz[e]); if (e & 1) { ow[e >> 1] = guard(pk2(tn[e - 1], tn[e])); PIN(ow[e >> 1]); } else PIN(tn[e]); }
    };
    const int f3v = (tbC * 16 + (lane < 14 ? lane : 13)) * 8;
    v2u f3, hr0, hr1;
#define C_TILE(t, CUR, PRV) do { \
      epi_consts(); \
      CUR = mfma32(csf, v4u{hh ? 0u : crn.x, hh ? 0u : crn.y, 0u, 0u}, f32x16{}); \
      cr = crn; \
      if ((t) < 3) crn = lds128(L, L_COR + ((t) + 1) * 512 + r32 * 16); \
      SCHED(); \
      _Pragma("unroll") for (int ks = 0; ks < 28; ++ks) { \
        CUR = mfma32(wc[ks], buf[ks % 4], CUR); \
        if ((t) == 1 && ks == 2) { PIN(CUR); wait4(L, SY_SV, 7); h_stage(1); F3_PUB(); } \
        if ((t) < 3 && ks == 23) { PIN(CUR); wait1(L, SY_H + ((t) + 1) * 4, 4); } \
        if ((t) < 3 || ks + 4 < 28) buf[ks % 4] = lds128(L, L_H + (t) * 28672 + (ks + 4) * 1024 + l16); \
        if (ks >= 2 && ks < 26) epi_step(PRV, ks - 2); \
        if (ks == 26) stc(rs, l16, ao + ((t) - 1) * 114688, v4u{ow[0], ow[1], ow[2], ow[3]}); \
        if ((t) == 1 && ks >= 4 && (ks % 4) == 0) wd[ks / 4 - 1] = ldc(rs, l16, WD_OFF(ks / 4 - 1)); \
        if ((t) == 2 && (ks % 3) == 0 && 6 + ks / 3 < NPRE) wd[6 + ks / 3] = ldc(rs, l16, WD_OFF(6 + ks / 3)); \
        if ((t) == 2 && (ks % 3) == 0 && 6 + ks / 3 >= NPRE && 6 + ks / 3 - NPRE < NLDS) dma_c(lb + L_WDL + (w * NLDS + 6 + ks / 3 - NPRE) * 1024, l16, rs, WD_OFF(28 - NLDS + 6 + ks / 3 - NPRE)); \
        if ((t) == 3 && ks == 0) { f3 = ldc2(rs, f3v, OFF_F3); \
          int sl = 4 * jj + (c8 >> 1) - 7 * rC; sl = sl < 0 ? sl + 28 : sl; \
          const int o_ = L_H + (rt ? tL : tE) * 28672 + sl * 1024 + tok * 16 + (c8 & 1) * 8; \
          hr0 = *(LAS v2u*)(L + o_); hr1 = *(LAS v2u*)(L + o_ + 512); } \
        if ((t) == 3 && (ks & 1) == 1 && NPRE + (ks >> 1) < 28 - NLDS) wd[NPRE + (ks >> 1)] = ldc(rs, l16, WD_OFF(NPRE + (ks >> 1))); \
        SCHED(); \
      } } while (0)
#define F3_PUB() do { VMW(0); publish_flag(SY_C3, OFF_F3 + (tbC * 16 + 7 * eC + jj) * 8, 7); } while (0)
    if (w >= 4) __builtin_amdgcn_s_setprio(CPRIO);
    C_TILE(1, accB, accA);
    C_TILE(2, accA, accB);
    ST(18);
    C_TILE(3, accB, accA);
    ST(19);
    epi_consts();
#pragma unroll
    for (int s = 0; s < 24; ++s) epi_step(accB, (s % 8) * 3 + s / 8);
    stc(rs, l16, ao + 3 * 114688, v4u{ow[0], ow[1], ow[2], ow[3]});
    SCHED();
    ST(20);
    flag_ensure(f3, rs, f3v, OFF_F3, tlo, thi);
    v4u af[14];
    const int aoE = OFF_ACT + (tbC * 4 + tE) * 114688, aoL = OFF_ACT + (tbC * 4 + tL) * 114688;
#define AF_OFF(n) (rot[(n) >> 1] + ((n) & 1) * 1024)
#define AF_ENSURE(LO, HI, BASE) do { unsigned m = 0; \
      _Pragma("unroll") for (int n = LO; n < HI; ++n) m = mx4(m, af[n]); \
      if (__builtin_expect(__any(m == SENT), 0)) { \
        _Pragma("unroll 1") for (unsigned sp = 0; sp < SPIN_CAP; ++sp) { \
          __builtin_amdgcn_s_sleep(RSLP); CFENCE(); m = 0; \
          _Pragma("unroll") for (int n = LO; n < HI; ++n) af[n] = ldc(rs, l16, (BASE) + AF_OFF(n)); \
          _Pragma("unroll") for (int n = LO; n < HI; ++n) m = mx4(m, af[n]); \
          if (!__any(m == SENT)) break; } } } while (0)
#pragma unroll
    for (int n = 0; n < 14; ++n) af[n] = ldc(rs, l16, aoE + AF_OFF(n));
    SCHED();
    VMW(15);
#pragma unroll
    for (int n = 0; n < NLDS; ++n) wd[28 - NLDS + n] = lds128(L, L_WDL + (w * NLDS + n) * 1024 + l16);
    arrive(L, SY_C2, lane);
    ST(21);
    auto red_out = [&](int ph, const f32x16& d0, const f32x16& d1) AI {
      const int ro = L_RED + (ph * 8 + w) * 4608 + r32 * 144 + h1 * 8;
#pragma unroll
      for (int g = 0; g < 4; ++g) {
        *(LAS v2u*)(L + ro + g * 16) = v2u{pk2(d0[4 * g], d0[4 * g + 1]), pk2(d0[4 * g + 2], d0[4 * g + 3])};
        *(LAS v2u*)(L + ro + 64 + g * 16) = v2u{pk2(d1[4 * g], d1[4 * g + 1]), pk2(d1[4 * g + 2], d1[4 * g + 3])};
      }
    };
    {
      f32x16 d0 = {}, d1 = {};
      AF_ENSURE(0, 6, aoE);
#pragma unroll
      for (int n = 0; n < 6; ++n) { d0 = mfma32(wd[n], af[n], d0); d1 = mfma32(wd[14 + n], af[n], d1); SCHED(); }
      PIN(d0); PIN(d1);
      AF_ENSURE(6, 14, aoE);
      wait1(L, SY_C2, 8);
#pragma unroll
      for (int n = 0; n < 6; ++n) af[n] = ldc(rs, l16, aoL + AF_OFF(n));
      SCHED();
#pragma unroll
      for (int n = 6; n < 14; ++n) { d0 = mfma32(wd[n], af[n], d0); d1 = mfma32(wd[14 + n], af[n], d1); af[n] = ldc(rs, l16, aoL + AF_OFF(n)); SCHED(); }
      red_out(0, d0, d1);
    }
    ST(22);
    {
      f32x16 d0 = {}, d1 = {};
      AF_ENSURE(0, 6, aoL);
#pragma unroll
      for (int n = 0; n < 6; ++n) { d0 = mfma32(wd[n], af[n], d0); d1 = mfma32(wd[14 + n], af[n], d1); SCHED(); }
      PIN(d0); PIN(d1);
      AF_ENSURE(6, 14, aoL);
      ST(23);
#pragma unroll
      for (int n = 6; n < 14; ++n) { d0 = mfma32(wd[n], af[n], d0); d1 = mfma32(wd[14 + n], af[n], d1); }
      red_out(1, d0, d1);
    }
    ST(24);
    bar();
    {
      float sm[8] = {lo_f(hr0.x), hi_f(hr0.x), lo_f(hr0.y), hi_f(hr0.y), lo_f(hr1.x), hi_f(hr1.x), lo_f(hr1.y), hi_f(hr1.y)};
      v4u v[8];
#pragma unroll
      for (int p = 0; p < 8; ++p) v[p] = lds128(L, L_RED + (rt * 8 + p) * 4608 + tok * 144 + c8 * 16);
#pragma unroll
      for (int p = 0; p < 8; ++p) ACC8(sm, v[p]);
      u16* op = a.out + (size_t)((tbC * 4 + (rt ? tL : tE)) * 32 + tok) * DM + 64 * jj + 8 * c8;
      *(GAS v4u*)op = v4u{pk2(sm[0], sm[1]), pk2(sm[2], sm[3]), pk2(sm[4], sm[5]), pk2(sm[6], sm[7])};
    }
  }
  asm volatile("hk_code_end:");
}
}

extern "C" void kernel_launch(void* const* d_in, const int* in_sizes, int n_in, void* d_out, int out_size, void* d_ws, size_t ws_size, hipStream_t stream) {
  using namespace hk;
  Args a;
  a.x = (const u16*)d_in[0]; a.wqkv = (const u16*)d_in[1]; a.wphi = (const u16*)d_in[2]; a.wo = (const u16*)d_in[3];
  a.wg = (const u16*)d_in[5]; a.wu = (const u16*)d_in[6]; a.wd = (const u16*)d_in[7];
  a.out = (u16*)d_out; a.ws = (char*)d_ws;
  (void)hipFuncSetAttribute((const void*)k_main, hipFuncAttributeMaxDynamicSharedMemorySize, LDS_BYTES);
  k_main<<<256, 512, LDS_BYTES, stream>>>(a);
}
```

```cpp
#include <hip/hip_runtime.h>

namespace hk {
typedef unsigned short u16;
typedef unsigned long long u64;
typedef unsigned v4u __attribute__((ext_vector_type(4)));
typedef unsigned v2u __attribute__((ext_vector_type(2)));
typedef float f32x16 __attribute__((ext_vector_type(16)));
typedef float f32x4 __attribute__((ext_vector_type(4)));
typedef float f32x2 __attribute__((ext_vector_type(2)));
typedef __bf16 bf16x8 __attribute__((ext_vector_type(8)));
typedef __bf16 bf16x2 __attribute__((ext_vector_type(2)));
typedef short s16x4 __attribute__((ext_vector_type(4)));
typedef __amdgpu_buffer_rsrc_t rsrc_t;
#define GAS __attribute__((address_space(1)))
#define LAS __attribute__((address_space(3)))
#define DI __device__ __forceinline__
#define __uint_as_float(u) __builtin_bit_cast(float, (unsigned)(u))
#define __float_as_uint(f) __builtin_bit_cast(unsigned, (float)(f))
#define __any(p) (__builtin_amdgcn_ballot_w64(p) != 0ull)
#define SCHED() __builtin_amdgcn_sched_barrier(0)
#define CFENCE() asm volatile("" ::: "memory")
#define VMW(n) asm volatile("s_waitcnt vmcnt(%0)" ::"i"(n) : "memory")

#define RSLP 2
#define FSLP 4
constexpr int SEQ = 2048, DM = 448, DFF = 1792;
constexpr int OFF_EPOCH = 0, OFF_F1 = 32768  , OFF_F3 = 65536  ;
constexpr int OFF_WD = 131072;
constexpr int OFF_KVP = OFF_WD + 14 * 112 * 1024;
constexpr int OFF_O = OFF_KVP + 16 * 16 * 4096;
constexpr int OFF_H = OFF_O + 64 * 16 * 2048;
constexpr int OFF_ACT = OFF_H + 64 * 28 * 1024;
constexpr int OFF_HS = OFF_ACT + 64 * 112 * 1024;
constexpr int OFF_END = OFF_HS + 64 * 1024;
constexpr unsigned SENT = 0xFFFFFFFFu;
constexpr unsigned SPIN_CAP = 1u << 15, LSPIN_CAP = 1u << 21;
constexpr int AUX_SC1 = 16;

constexpr int L_X = 0;
constexpr int L_WK = 114688;
constexpr int L_RS = 143360;
constexpr int L_WPHI = 159744;
constexpr int L_SY = 161600;
constexpr int L_COR = 161792;
constexpr int L_JUNK = L_WPHI + 1600;
constexpr int L_WV = 0, L_WQ = 28672;
constexpr int L_PHI = L_RS;
constexpr int L_V = 4 * 16384;
constexpr int L_KV = 5 * 16384;
constexpr int L_OIMG = 57344;
constexpr int L_OIM2 = 143360;
constexpr int L_BK = 86016;
constexpr int L_BV = 0, L_BV2 = 28672;
#define HB(t) ((t) == 0 ? 0 : (t) == 1 ? 86016 : (t) == 2 ? 28672 : 57344)
constexpr int L_ST2 = 147456;
constexpr int L_H = 0;
constexpr int L_WDL = 114688;
constexpr int L_RED = 0;
constexpr int LDS_BYTES = 163840;
constexpr int SY_VP = L_SY, SY_KP = L_SY + 16, SY_SK = L_SY + 32, SY_SV = L_SY + 48, SY_H = L_SY + 64  , SY_C1 = L_SY + 80, SY_C2 = L_SY + 84, SY_C3 = L_SY + 88, SY_S = L_SY + 92, SY_G = L_SY + 96  ;

DI unsigned pk2(float a, float b) { f32x2 v = {a, b}; return __builtin_bit_cast(unsigned, __builtin_convertvector(v, bf16x2)); }
DI float lo_f(unsigned u) { return __uint_as_float(u << 16); }
DI float hi_f(unsigned u) { return __uint_as_float(u & 0xffff0000u); }
DI float dot2(unsigned a, unsigned b, float c) { return __builtin_amdgcn_fdot2_f32_bf16(__builtin_bit_cast(bf16x2, a), __builtin_bit_cast(bf16x2, b), c, false); }
DI f32x16 mfma32(v4u a, v4u b, f32x16 c) { return __builtin_amdgcn_mfma_f32_32x32x16_bf16(__builtin_bit_cast(bf16x8, a), __builtin_bit_cast(bf16x8, b), c, 0, 0, 0); }
DI f32x4 mfma16(v4u a, v4u b, f32x4 c) { return __builtin_amdgcn_mfma_f32_16x16x32_bf16(__builtin_bit_cast(bf16x8, a), __builtin_bit_cast(bf16x8, b), c, 0, 0, 0); }
DI void bar() { asm volatile("s_waitcnt lgkmcnt(0)" ::: "memory"); __builtin_amdgcn_s_barrier(); CFENCE(); }
DI float xsum(float v) { unsigned u = __float_as_uint(v); auto r = __builtin_amdgcn_permlane32_swap(u, u, false, false); return __uint_as_float(r[0]) + __uint_as_float(r[1]); }
DI float xmax(float v) { unsigned u = __float_as_uint(v); auto r = __builtin_amdgcn_permlane32_swap(u, u, false, false); return fmaxf(__uint_as_float(r[0]), __uint_as_float(r[1])); }
DI float xmin(float v) { unsigned u = __float_as_uint(v); auto r = __builtin_amdgcn_permlane32_swap(u, u, false, false); return fminf(__uint_as_float(r[0]), __uint_as_float(r[1])); }
DI float upper(float v) { unsigned u = __float_as_uint(v); auto r = __builtin_amdgcn_permlane32_swap(u, u, false, false); return __uint_as_float(r[1]); }
DI unsigned guard(unsigned u) { return u < 0xFFFFFFFEu ? u : 0xFFFFFFFEu; }
DI v4u guard4(v4u v) { return v4u{guard(v.x), guard(v.y), guard(v.z), guard(v.w)}; }
DI unsigned mx4(unsigned m, v4u a) { m = max(m, max(a.x, a.y)); return max(m, max(a.z, a.w)); }
DI v4u ldc(rsrc_t rs, int voff, int soff) { return __builtin_amdgcn_raw_buffer_load_b128(rs, voff, soff, AUX_SC1); }
DI void stc(rsrc_t rs, int voff, int soff, v4u v) { __builtin_amdgcn_raw_buffer_store_b128(v, rs, voff + soff, 0, AUX_SC1); }
DI v4u lds128(LAS char* L, int off) { return *(LAS v4u*)(L + off); }
DI v2u ldtr(LAS char* L, int off) { return __builtin_bit_cast(v2u, __builtin_amdgcn_ds_read_tr16_b64_v4i16((LAS s16x4*)(L + off))); }
DI v4u pack8(const f32x16& a, int s) { return v4u{pk2(a[8 * s], a[8 * s + 1]), pk2(a[8 * s + 2], a[8 * s + 3]), pk2(a[8 * s + 4], a[8 * s + 5]), pk2(a[8 * s + 6], a[8 * s + 7])}; }
DI void dma(int lds, int voff, rsrc_t r, int soff) {
  asm volatile("s_mov_b32 m0, %0\n\ts_nop 0\n\tbuffer_load_dwordx4 %1, %2, %3 offen lds" ::"s"(lds), "v"(voff), "s"(r), "s"(soff) : "memory");
}
DI void dma_c(int lds, int voff, rsrc_t r, int soff) {
  asm volatile("s_mov_b32 m0, %0\n\ts_nop 0\n\tbuffer_load_dwordx4 %1, %2, %3 offen sc1 lds" ::"s"(lds), "v"(voff), "s"(r), "s"(soff) : "memory");
}
DI void touch(int lds, int voff, rsrc_t r, int soff) {
  asm volatile("s_mov_b32 m0, %0\n\ts_nop 0\n\tbuffer_load_dword %1, %2, %3 offen lds" ::"s"(lds), "v"(voff), "s"(r), "s"(soff) : "memory");
}
template <int OFF, int N> DI void swarm(unsigned& junk, const void* base) {
  if constexpr (N > 0) {
    asm volatile("s_load_dword %0, %1, %2" : "+s"(junk) : "s"(base), "i"(OFF));
    swarm<OFF + 128, N - 1>(junk, base);
  }
}
DI void dma_nt(int lds, int voff, rsrc_t r, int soff) {
  asm volatile("s_mov_b32 m0, %0\n\ts_nop 0\n\tbuffer_load_dwordx4 %1, %2, %3 offen nt lds" ::"s"(lds), "v"(voff), "s"(r), "s"(soff) : "memory");
}
DI v2u ldc2(rsrc_t rs, int voff, int soff) { return __builtin_amdgcn_raw_buffer_load_b64(rs, voff, soff, AUX_SC1); }
DI void stflag(rsrc_t rs, int off, unsigned tlo, unsigned thi) { __builtin_amdgcn_raw_buffer_store_b64(v2u{tlo, thi}, rs, off, 0, AUX_SC1); }
DI void flag_ensure(v2u f, rsrc_t rs, int voff, int soff, unsigned tlo, unsigned thi) {
  if (__builtin_expect(__any(((f.x ^ tlo) | (f.y ^ thi)) != 0), 0)) {
#pragma unroll 1
    for (unsigned sp = 0; sp < SPIN_CAP; ++sp) {
      __builtin_amdgcn_s_sleep(FSLP);
      CFENCE();
      f = ldc2(rs, voff, soff);
      if (!__any(((f.x ^ tlo) | (f.y ^ thi)) != 0)) break;
    }
  }
  CFENCE();
}
DI void sig(LAS char* L, int off, unsigned v, int) {
  asm volatile("s_mov_b64 exec, 1\n\tds_write_b32 %0, %1\n\ts_mov_b64 exec, -1" ::"v"((unsigned)(size_t)(L + off)), "v"(v) : "memory");
}
DI void arrive(LAS char* L, int off, int) {
  asm volatile("s_mov_b64 exec, 1\n\tds_add_u32 %0, %1\n\ts_mov_b64 exec, -1" ::"v"((unsigned)(size_t)(L + off)), "v"(1u) : "memory");
}
#define PIN(x) asm volatile("" : "+v"(x))
DI unsigned min4u(v4u v) { return (unsigned)__builtin_amdgcn_readfirstlane(min(min(v.x, v.y), min(v.z, v.w))); }
DI void wait4(LAS char* L, int off, unsigned target) {
#pragma unroll 1
  for (unsigned sp = 0; sp < LSPIN_CAP; ++sp) {
    v4u v = *(volatile LAS v4u*)(L + off);
    if ((unsigned)__builtin_amdgcn_readfirstlane(min(min(v.x, v.y), min(v.z, v.w))) >= target) break;
    __builtin_amdgcn_s_sleep(1);
  }
  CFENCE();
}
DI void wait8(LAS char* L, unsigned vt, unsigned kt) {
#pragma unroll 1
  for (unsigned sp = 0; sp < LSPIN_CAP; ++sp) {
    v4u v = *(volatile LAS v4u*)(L + SY_VP), k = *(volatile LAS v4u*)(L + SY_KP);
    if (min4u(v) >= vt && min4u(k) >= kt) break;
    __builtin_amdgcn_s_sleep(1);
  }
  CFENCE();
}
DI void wait1(LAS char* L, int off, unsigned target) {
#pragma unroll 1
  for (unsigned sp = 0; sp < LSPIN_CAP; ++sp) {
    unsigned v = *(volatile LAS unsigned*)(L + off);
    if ((unsigned)__builtin_amdgcn_readfirstlane(v) >= target) break;
    __builtin_amdgcn_s_sleep(1);
  }
  CFENCE();
}

template <int V> struct IC { static constexpr int v = V; };
template <int I, int N, class F> DI void sfor(F&& f) { if constexpr (I < N) { f(IC<I>{}); sfor<I + 1, N>(f); } }
template <int NK, int P, bool REG_IS_A, class H> DI f32x16 gemm_hook(const v4u (&reg)[NK], LAS char* L, int base, f32x16 acc, H&& hook) {
  v4u buf[P];
#pragma unroll
  for (int i = 0; i < P; ++i) buf[i] = lds128(L, base + i * 1024);
  SCHED();
  sfor<0, NK>([&](auto I) __attribute__((always_inline)) {
    constexpr int ks = decltype(I)::v;
    acc = REG_IS_A ? mfma32(reg[ks], buf[ks % P], acc) : mfma32(buf[ks % P], reg[ks], acc);
    if (ks + P < NK) buf[ks % P] = lds128(L, base + (ks + P) * 1024);
    hook(I);
    SCHED();
  });
  PIN(acc);
  return acc;
}
template <int NK, int P, bool REG_IS_A> DI f32x16 gemm_lds(const v4u (&reg)[NK], LAS char* L, int base, f32x16 acc) {
  v4u buf[P];
#pragma unroll
  for (int i = 0; i < P; ++i) buf[i] = lds128(L, base + i * 1024);
  SCHED();
#pragma unroll
  for (int ks = 0; ks < NK; ++ks) {
    acc = REG_IS_A ? mfma32(reg[ks], buf[ks % P], acc) : mfma32(buf[ks % P], reg[ks], acc);
    if (ks + P < NK) buf[ks % P] = lds128(L, base + (ks + P) * 1024);
    SCHED();
  }
  PIN(acc);
  return acc;
}
template <int NK, int P> DI f32x16 gemm_cs(const v4u (&reg)[NK], LAS char* L, int base, f32x4& c4, unsigned ONES) {
  v4u buf[P];
  f32x16 acc = {};
#pragma unroll
  for (int i = 0; i < P; ++i) buf[i] = lds128(L, base + i * 1024);
  SCHED();
#pragma unroll
  for (int ks = 0; ks < NK; ks += 2) {
    const v4u b0 = buf[ks % P], b1 = buf[(ks + 1) % P];
    c4.x = dot2(b0.x, ONES, c4.x); c4.y = dot2(b0.y, ONES, c4.y); c4.z = dot2(b0.z, ONES, c4.z); c4.w = dot2(b0.w, ONES, c4.w);
    c4.x = dot2(b1.x, ONES, c4.x); c4.y = dot2(b1.y, ONES, c4.y); c4.z = dot2(b1.z, ONES, c4.z); c4.w = dot2(b1.w, ONES, c4.w);
    SCHED();
    acc = mfma32(b0, reg[ks], acc);
    acc = mfma32(b1, reg[ks + 1], acc);
    if (ks + P < NK) { buf[ks % P] = lds128(L, base + (ks + P) * 1024); buf[(ks + 1) % P] = lds128(L, base + (ks + 1 + P) * 1024); }
    SCHED();
  }
  PIN(acc);
  return acc;
}
template <int LO, int HI, int STRIDE, int N> DI void issue(v4u (&a)[N], rsrc_t rs, int voff, int soff) {
#pragma unroll
  for (int i = LO; i < HI; ++i) a[i] = ldc(rs, voff, soff + i * STRIDE);
}
template <int LO, int HI, int STRIDE, int N> DI void ensure(v4u (&a)[N], rsrc_t rs, int voff, int soff) {
  unsigned m = 0;
#pragma unroll
  for (int i = LO; i < HI; ++i) m = mx4(m, a[i]);
  if (__builtin_expect(__any(m == SENT), 0)) {
#pragma unroll 1
    for (unsigned sp = 0; sp < SPIN_CAP; ++sp) {
      __builtin_amdgcn_s_sleep(RSLP);
      CFENCE();
      issue<LO, HI, STRIDE>(a, rs, voff, soff);
      m = 0;
#pragma unroll
      for (int i = LO; i < HI; ++i) m = mx4(m, a[i]);
      if (!__any(m == SENT)) break;
    }
  }
}
DI void flags_wait(rsrc_t rs, int off, int lane, unsigned tlo, unsigned thi) {
#pragma unroll 1
  for (unsigned sp = 0; sp < SPIN_CAP; ++sp) {
    CFENCE();
    v4u a = ldc(rs, lane * 16, off), b = ldc(rs, lane * 16, off + 1024);
    unsigned d = (a.x ^ tlo) | (a.z ^ tlo) | (b.x ^ tlo) | (b.z ^ tlo) | (a.y ^ thi) | (a.w ^ thi) | (b.y ^ thi) | (b.w ^ thi);
    if (!__any(d != 0)) break;
  }
  CFENCE();
}
DI void delay(int n) { for (int i = 0; i < n; ++i) __builtin_amdgcn_s_sleep(1); }

#define ST(k)

struct Args { const u16 *x, *wqkv, *wphi, *wo, *wg, *wu, *wd; u16* out; char* ws; };

constexpr float L2E = 1.4426950408889634f;
#define SUM4(a, v) do { a.x = dot2(v.x, ONES, a.x); a.y = dot2(v.y, ONES, a.y); a.z = dot2(v.z, ONES, a.z); a.w = dot2(v.w, ONES, a.w); } while (0)
#define SQ4(a, v) do { a.x = dot2(v.x, v.x, a.x); a.y = dot2(v.y, v.y, a.y); a.z = dot2(v.z, v.z, a.z); a.w = dot2(v.w, v.w, a.w); } while (0)
#define HS4(a) ((a.x + a.y) + (a.z + a.w))
#define ACC8(sm, v) do { sm[0] = dot2(v.x, SEL_LO, sm[0]); sm[1] = dot2(v.x, SEL_HI, sm[1]); sm[2] = dot2(v.y, SEL_LO, sm[2]); sm[3] = dot2(v.y, SEL_HI, sm[3]); \
    sm[4] = dot2(v.z, SEL_LO, sm[4]); sm[5] = dot2(v.z, SEL_HI, sm[5]); sm[6] = dot2(v.w, SEL_LO, sm[6]); sm[7] = dot2(v.w, SEL_HI, sm[7]); } while (0)
DI unsigned hilo(float v) { unsigned h = pk2(v, 0.f) & 0xffffu; float r = v - lo_f(h); return h | (pk2(r, 0.f) << 16); }

template <class H> DI void feature_map(const f32x16& acc, float rstd, const v4u (&wp)[2], bool hh, f32x16& ep, f32x16& en, H&& hook) {
  f32x16 t = acc * rstd;
  v4u t0 = pack8(t, 0), t1 = pack8(t, 1);
  if (hh) { t1.z = 0; t1.w = 0; }
  f32x16 p = {};
  p = mfma32(wp[0], t0, p);
  p = mfma32(wp[1], t1, p);
  hook(IC<0>{}); SCHED();
  float mx = fmaxf(fmaxf(p[12], p[13]), fmaxf(p[14], p[15])), mn = fminf(fminf(p[12], p[13]), fminf(p[14], p[15]));
  mx = hh ? -__builtin_inff() : mx; mn = hh ? __builtin_inff() : mn;
#pragma unroll
  for (int i = 0; i < 12; i += 2) { mx = fmaxf(fmaxf(mx, p[i]), p[i + 1]); mn = fminf(fminf(mn, p[i]), p[i + 1]); }
  mx = xmax(mx); mn = xmin(mn);
  hook(IC<1>{}); SCHED();
  float bp = -mx * L2E, bn = mn * L2E;
  float bpp = hh ? -__builtin_inff() : bp, bnp = hh ? -__builtin_inff() : bn;
  float sp0 = 0.f, sp1 = 0.f, sn0 = 0.f, sn1 = 0.f;
  sfor<0, 4>([&](auto Q) __attribute__((always_inline)) {
    constexpr int q = decltype(Q)::v;
#pragma unroll
    for (int i = 4 * q; i < 4 * q + 4; ++i) {
      ep[i] = __builtin_amdgcn_exp2f(__builtin_fmaf(p[i], L2E, i < 12 ? bp : bpp));
      en[i] = __builtin_amdgcn_exp2f(__builtin_fmaf(p[i], -L2E, i < 12 ? bn : bnp));
    }
    if (q > 0) {
      sp0 += ep[4 * q - 4] + ep[4 * q - 3]; sp1 += ep[4 * q - 2] + ep[4 * q - 1]; sn0 += en[4 * q - 4] + en[4 * q - 3]; sn1 += en[4 * q - 2] + en[4 * q - 1];
    }
    hook(IC<2 + q>{}); SCHED();
  });
  sp0 += ep[12] + ep[13]; sp1 += ep[14] + ep[15]; sn0 += en[12] + en[13]; sn1 += en[14] + en[15];
  float rp = __builtin_amdgcn_rcpf(xsum(sp0 + sp1)), rn = __builtin_amdgcn_rcpf(xsum(sn0 + sn1));
  hook(IC<6>{}); SCHED();
  ep *= rp;
  hook(IC<7>{}); SCHED();
  en *= rn;
}

DI void retile_out(LAS char* L, rsrc_t rs, int l16, int a0, int au, int aj0, int aj1, int d0, int dj0, int dj1, bool mlo, bool mhi) {
  v2u lo[4], hi[4];
#pragma unroll
  for (int j = 0; j < 4; ++j) { lo[j] = ldtr(L, a0 + (j & 1) * aj0 + (j >> 1) * aj1); hi[j] = ldtr(L, a0 + au + (j & 1) * aj0 + (j >> 1) * aj1); }
#pragma unroll
  for (int j = 0; j < 4; ++j) {
    v4u f = {mlo ? 0u : lo[j].x, mlo ? 0u : lo[j].y, mhi ? 0u : hi[j].x, mhi ? 0u : hi[j].y};
    stc(rs, l16, d0 + (j & 1) * dj0 + (j >> 1) * dj1, f);
  }
}

template <int NK, int P, class H> DI f32x16 gemm_tr(const v4u (&reg)[NK], LAS char* L, int base, H&& hook) {
  v2u bl[P], bh[P];
  f32x16 acc = {};
#pragma unroll
  for (int i = 0; i < P; ++i) { bl[i] = ldtr(L, base + i * 1024); bh[i] = ldtr(L, base + i * 1024 + 256); }
  SCHED();
  sfor<0, NK>([&](auto I) __attribute__((always_inline)) {
    constexpr int ks = decltype(I)::v;
    acc = mfma32(v4u{bl[ks % P].x, bl[ks % P].y, bh[ks % P].x, bh[ks % P].y}, reg[ks], acc);
    if (ks + P < NK) { bl[ks % P] = ldtr(L, base + (ks + P) * 1024); bh[ks % P] = ldtr(L, base + (ks + P) * 1024 + 256); }
    hook(I);
    SCHED();
  });
  PIN(acc);
  return acc;
}
#define SWARM 1
#define VW1 7
#define KDLY 0
#define ODLY 4
#define HDLY 4
#define CPRIO 1
#define KE 0
#if defined(__HIP_DEVICE_COMPILE__)
#define NOPK __attribute__((target("no-packed-fp32-ops")))
#else
#define NOPK
#endif
#define AI __attribute__((always_inline))
__global__ void __launch_bounds__(512, 2) NOPK k_main(Args a) {
  extern __shared__ __attribute__((aligned(16))) char smem[];
  LAS char* L = (LAS char*)smem;
  const int lb = (int)(unsigned)(size_t)L;
  const int b = blockIdx.x, xcd = b & 7, ci = b >> 3;
  const int tid = threadIdx.x, lane = tid & 63, w = __builtin_amdgcn_readfirstlane(tid >> 6);
  ST(1);
  if (w == 7) {
    u64 pc; unsigned len;
    asm volatile("s_getpc_b64 %0\nhk_code_begin:\n\ts_mov_b32 %1, hk_code_end-hk_code_begin" : "=s"(pc), "=s"(len));
    touch(lb + L_JUNK, ((ci & 7) * 64 + lane) * 128, __builtin_amdgcn_make_buffer_rsrc((void*)pc, 0, len, 0x00020000), 0);
  }
  if (w == 3 && lane < 48) *(LAS unsigned*)(L + L_SY + lane * 4) = 0u;
  const rsrc_t rqkv = __builtin_amdgcn_make_buffer_rsrc((void*)a.wqkv, 0, DM * 3 * DM * 2, 0x00020000);
  const rsrc_t rx = __builtin_amdgcn_make_buffer_rsrc((void*)a.x, 0, SEQ * DM * 2, 0x00020000);
  const int t4 = w & 3, G = w >> 2;
  const int tbA = 4 * (xcd >> 1) + (ci >> 3), hA = 8 * (xcd & 1) + (ci & 7);
  const int ksh = hA == 15 ? 8 : 0;
  const int xr_ = lane >> 3;
  const int ve = xr_ * 896 + (((lane & 7) ^ (xr_ >> 1)) * 16), vod = ve ^ 64;
  const int vk = (lane >> 2) * 2688 + (lane & 3) * 16;
  const int sx = (tbA * 128 + 32 * t4) * 896, sk = 16 * t4 * 2688 + 56 * hA - ksh;
  auto seg_issue = [&](int s) AI {
    dma(lb + L_WK + (64 * s + 16 * t4) * 64, vk, rqkv, sk + 896 + 64 * s * 2688);
#pragma unroll
    for (int i = 0; i < 4; ++i) dma(lb + L_X + s * 16384 + (32 * t4 + 8 * i) * 128, (i & 1) ? vod : ve, rx, sx + 8 * i * 896 + s * 128);
  };
  ST(25);
  if (w < 2) { if (lane < 56) touch(lb + L_JUNK, (ci * 112 + w * 56 + lane) * 128, rx, (xcd >> 1) * 512 * 896); }
  else if (w < 4) { const int j = ci * 56 + lane; if (lane < 56) touch(lb + L_JUNK, (j >> 2) * 2688 + (j & 3) * 128, rqkv, (7 * (w - 1) + 3 * (xcd & 1)) * 128); }
  else { seg_issue(0); }
  ST(26);
  bar();
  int lane_ = lane, b_ = b;
  asm volatile("" : "+v"(lane_), "+s"(b_));
#define lane lane_
  const int xcd2 = b_ & 7, ci2 = b_ >> 3;
  const int r32 = lane & 31; const bool hh = lane >= 32; const int h1 = lane >> 5;
  const int q4 = (lane & 15) >> 2, p4 = lane & 3, blk = (lane >> 4) & 1, g4 = lane >> 4, c16 = lane & 15;
  const rsrc_t rs = __builtin_amdgcn_make_buffer_rsrc(a.ws, 0, OFF_END, 0x00020000);
  const int l16 = lane * 16;
  unsigned ONES, SEL_LO, SEL_HI;
  asm volatile("s_mov_b32 %0, 0x3f803f80\n\ts_mov_b32 %1, 0x3f80\n\ts_mov_b32 %2, 0x3f800000" : "=s"(ONES), "=s"(SEL_LO), "=s"(SEL_HI));
  const int ttB = 8 * xcd2 + (ci2 >> 2), cgB = ci2 & 3;
  const int ntB = 8 * cgB + w;
  const bool actB = ntB < 28;
  const bool actC = ci2 < 28;
  const int cc = actC ? ci2 : 27;
  const int rC = cc / 7, tbC = 4 * (xcd2 >> 1) + rC, jj = cc % 7, eC = xcd2 & 1;
  const int TC0 = 8 * (7 * eC + jj), TC = TC0 + w;
  const v4u sent4 = {SENT, SENT, SENT, SENT};
  const u64 tag0 = *(const __attribute__((address_space(4))) u64*)(a.ws + OFF_EPOCH) + 1ull;
  unsigned tlo = (unsigned)tag0, thi = (unsigned)(tag0 >> 32);
  asm volatile("" : "+s"(tlo), "+s"(thi));

  v4u wc[28], wo[16], wp[2], hs[7], csf;
  v2u xr[2], q0, q1;
  f32x4 c4c = {};
  const int hsv = r32 * 8 + h1 * 512, hss = OFF_HS + (tbC * 4 + t4) * 1024;
  const rsrc_t rwc = __builtin_amdgcn_make_buffer_rsrc((void*)(t4 < 2 ? a.wg : a.wu), 0, actC ? DM * DFF * 2 : 0, 0x00020000);
  const int wcv = (lane >> 3) * 3584 + (((((lane & 7) >> 1) ^ (((lane >> 4) & 1) | (t4 & 2))) << 1) | (lane & 1)) * 16;
  const int wcs = (t4 & 1) * 56 * 3584 + TC0 * 32 + G * 128;
  const int lst = lb + t4 * 7168;
#define WC1(buf, i) dma(lst + (buf) + (((i) % 14) / 7) * 28672 + ((i) % 7) * 1024, wcv, rwc, wcs + (((((i) / 7) + rC) & 3) * 112 + ((i) % 7) * 8) * 3584)
  const rsrc_t rwo = __builtin_amdgcn_make_buffer_rsrc((void*)a.wo, 0, actB ? DM * DM * 2 : 0, 0x00020000);
  const int wov = (lane >> 3) * 896 + (((((lane & 7) >> 1) ^ ((lane >> 4) & 3)) << 1) | (lane & 1)) * 16;
  const int wos = t4 * 56 * 896 + cgB * 256 + G * 128;
#define WO1(buf, i) dma(lst + (buf) + ((i) / 7) * 28672 + ((i) % 7) * 1024, wov, rwo, wos + (((i) / 7) * 224 + ((i) % 7) * 8) * 896)
#define WC_B(buf, LO, HI) do { _Pragma("unroll") for (int i = LO; i < HI; ++i) WC1(buf, i); } while (0)
#define WO_B(buf, LO, HI) do { _Pragma("unroll") for (int i = LO; i < HI; ++i) WO1(buf, i); } while (0)
#define XR_LOAD() do { const u16* xp = a.x + (size_t)(32 * ttB + c16) * DM + 16 * (actB ? ntB : 0) + 4 * g4; xr[0] = *(const GAS v2u*)xp; xr[1] = *(const GAS v2u*)(xp + 16 * DM); SCHED(); } while (0)
  const int sy_s = (G ? SY_SV : SY_SK);
#define LANDED(n) sig(L, sy_s + t4 * 4, 2 * (n) + 2, lane)
  auto rd_wo = [&](int buf) AI {
    const int r0 = 16 * (g4 >> 1) + 4 * (g4 & 1) + q4;
    const int ae = buf + r0 * 128 + ((t4 ^ ((r0 >> 1) & 3)) * 32) + p4 * 8, ao_ = buf + r0 * 128 + ((t4 ^ (((r0 >> 1) + 2) & 3)) * 32) + p4 * 8;
    wait4(L, sy_s, 2);
#pragma unroll
    for (int hd = 0; hd < 16; ++hd) {
      const int base = ((hd & 1) ? ao_ : ae) + hd * 28 * 128;
      v2u lo = ldtr(L, base), hi = ldtr(L, base + 1024);
      if (g4 == 3) hi = v2u{0, 0};
      wo[hd] = v4u{lo.x, lo.y, hi.x, hi.y};
    }
    sig(L, sy_s + t4 * 4, 3, lane);
#pragma unroll
    for (int hd = 0; hd < 16; ++hd) PIN(wo[hd]);
  };
  auto rd_wc = [&](auto HALF, int buf) AI {
    constexpr int o = 14 * decltype(HALF)::v, n = 1 + decltype(HALF)::v;
    const int ad = buf + blk * 14336 + (4 * h1 + q4) * 128 + ((t4 ^ ((q4 >> 1) | (blk << 1))) * 32) + p4 * 8;
    wait4(L, sy_s, 2 * n + 2);
#pragma unroll
    for (int i = 0; i < 14; ++i) {
      v2u lo = ldtr(L, ad + (i / 7) * 28672 + (i % 7) * 2048), hi = ldtr(L, ad + (i / 7) * 28672 + (i % 7) * 2048 + 1024);
      wc[o + i] = v4u{lo.x, lo.y, hi.x, hi.y};
    }
    sig(L, sy_s + t4 * 4, 2 * n + 3, lane);
#pragma unroll
    for (int i = 0; i < 14; ++i) { SUM4(c4c, wc[o + i]); PIN(wc[o + i]); }
    PIN(c4c);
  };
  auto load_wp = [&]() AI {
#pragma unroll
    for (int s = 0; s < 2; ++s) {
      v2u lo = ldtr(L, L_WPHI + (16 * s + 4 * h1 + q4) * 56 + blk * 32 + p4 * 8);
      v2u hi = ldtr(L, L_WPHI + (16 * s + 8 + 4 * h1 + q4) * 56 + blk * 32 + p4 * 8);
      if (s == 1 && hh) hi = v2u{0, 0};
      if (r32 >= 28) { lo = v2u{0, 0}; hi = v2u{0, 0}; }
      wp[s] = v4u{lo.x, lo.y, hi.x, hi.y};
    }
    PIN(wp[0]); PIN(wp[1]);
  };
  auto publish_flag = [&](int cnt_off, int foff, unsigned last) AI {
    CFENCE();
    if (lane == 0) {
      unsigned old = __hip_atomic_fetch_add((LAS unsigned*)(L + cnt_off), 1u, __ATOMIC_RELAXED, __HIP_MEMORY_SCOPE_WORKGROUP);
      if (old == last) stflag(rs, foff, tlo, thi);
    }
    CFENCE();
  };
  const int slotB = (g4 & 1) * 32 + (hh ? 16 : 0) + c16;
  auto phase_b2 = [&](auto hook) AI {
    wait4(L, SY_KP, 11);
    ST(12);
    const int bo = (g4 >> 1) * 1024 + ((g4 & 1) * 32 + c16) * 16;
#define OB(i) (bo + (((i) >> 1) < 14 ? L_OIMG + ((i) >> 1) * 2048 : L_OIM2 + (((i) >> 1) - 14) * 2048) + ((i) & 1) * 256)
    f32x4 h0 = {}, hq = {};
    v4u ob[8];
#pragma unroll
    for (int i = 0; i < 8; ++i) ob[i] = lds128(L, OB(i));
    SCHED();
    sfor<0, 32>([&](auto I) AI {
      constexpr int i = decltype(I)::v;
      if (i & 1) hq = mfma16(wo[i >> 1], ob[i % 8], hq); else h0 = mfma16(wo[i >> 1], ob[i % 8], h0);
      if (i + 8 < 32) ob[i % 8] = lds128(L, OB(i + 8));
      hook(I);
      SCHED();
    });
    unsigned a0 = pk2(h0.x + lo_f(xr[0].x), h0.y + hi_f(xr[0].x)), a1 = pk2(h0.z + lo_f(xr[0].y), h0.w + hi_f(xr[0].y));
    unsigned b0 = pk2(hq.x + lo_f(xr[1].x), hq.y + hi_f(xr[1].x)), b1 = pk2(hq.z + lo_f(xr[1].y), hq.w + hi_f(xr[1].y));
    auto s0 = __builtin_amdgcn_permlane32_swap(a0, b0, false, false);
    auto s1 = __builtin_amdgcn_permlane32_swap(a1, b1, false, false);
    const v4u hv = guard4(v4u{s0[0], s1[0], s0[1], s1[1]});
    stc(rs, slotB * 16, actB ? OFF_H + (ttB * 28 + ntB) * 1024 : OFF_END, hv);
    SCHED();
    f32x4 a1_ = {}, a2_ = {};
    SUM4(a1_, hv); SQ4(a2_, hv);
    *(LAS f32x2*)(L + L_ST2 + (slotB & 31) * 144 + (slotB >> 5) * 64 + w * 8) = f32x2{HS4(a1_), HS4(a2_)};
    arrive(L, SY_S, lane);
    ST(13);
  };
  const int hso = OFF_H + tbC * 114688 + G * 57344 + ((t4 + rC) & 3) * 7168;
  const int hlo = t4 * 7168 + l16;
  auto h_stage = [&](v4u (&hx)[7], int r) AI {
    ensure<0, 7, 1024>(hx, rs, l16, hso + r * 28672);
#pragma unroll
    for (int i = 0; i < 7; ++i) *(LAS v4u*)(L + hlo + (G ? HB(2 + r) : HB(r)) + i * 1024) = hx[i];
    arrive(L, SY_H + (2 * G + r) * 4, lane);
  };

  if (w < 4) {
    const int lrs = L_RS + w * 4096;
    const int mR = rC * 8 + xcd2;
    const bool hasR = actC && w < 2 && mR < 28;
    const int T2 = 2 * mR + w, c64 = jj;
    if (hasR) {
      const rsrc_t rd = __builtin_amdgcn_make_buffer_rsrc((void*)a.wd, 0, DM * DFF * 2, 0x00020000);
      const int vo = (lane >> 3) * 896 + (lane & 7) * 16;
#pragma unroll
      for (int i = 0; i < 4; ++i) dma(lb + lrs + i * 1024, vo, rd, (32 * T2 + 8 * i) * 896 + 128 * c64);
    }
    if (w >= 2) {
      const rsrc_t rp = __builtin_amdgcn_make_buffer_rsrc((void*)a.wphi, 0, 16 * 28 * 28 * 2, 0x00020000);
      if ((w - 2) * 64 + lane < 98) dma(lb + L_WPHI + (w - 2) * 1024, l16, rp, hA * 1568 + (w - 2) * 1024);
    }
    VMW(0);
    ST(2);
    if (hasR) retile_out(L, rs, l16, lrs + (4 * h1 + q4) * 128 + blk * 32 + p4 * 8, 1024, 64, 2048, OFF_WD + (2 * c64 * 112 + 2 * T2) * 1024, 114688, 1024, false, false);
    const int pf0 = OFF_KVP + (hA * 16 + tbA) * 4096 + w * 1024, pf1 = OFF_O + ((tbA * 4 + w) * 16 + hA) * 2048;
    const int pf3 = OFF_H + (ttB * 28 + 8 * cgB + w) * 1024, pf4 = cgB < 3 ? pf3 + 4096 : OFF_END;
    const int pf5 = (w == 3 && lane < 16) ? OFF_HS + (ttB * 4 + cgB) * 256 : OFF_END;
    ST(3);
    f32x16 acc = {};
    f32x4 c4 = {};
    {
      const int row = 32 * w + r32, key = (row >> 1) & 7;
      int xa[4];
#pragma unroll
      for (int i = 0; i < 4; ++i) xa[i] = L_X + row * 128 + (((2 * i + h1) ^ key) * 16);
      const int wa = L_WK + (8 * h1 + q4) * 64 + blk * 32 + p4 * 8 + ksh;
      unsigned prog = 0;
      sfor<0, 7>([&](auto S) AI {
        constexpr int s = decltype(S)::v;
        if (prog <= (unsigned)s) wait4(L, SY_VP, s + 1);
        v4u xb[4]; v2u wl[4], wh[4];
#pragma unroll
        for (int i = 0; i < 4; ++i) { wl[i] = ldtr(L, wa + s * 4096 + i * 1024); wh[i] = ldtr(L, wa + s * 4096 + i * 1024 + 256); xb[i] = lds128(L, xa[i] + s * 16384); }
        SCHED();
#pragma unroll
        for (int i = 0; i < 4; ++i) { v4u wf = {wl[i].x, wl[i].y, wh[i].x, wh[i].y}; acc = mfma32(wf, xb[i], acc); }
        v4u pv = *(volatile LAS v4u*)(L + SY_VP);
        SCHED();
#pragma unroll
        for (int i = 0; i < 4; ++i) { v4u wf = {wl[i].x, wl[i].y, wh[i].x, wh[i].y}; SUM4(c4, wf); }
        PIN(acc); PIN(c4);
        prog = min4u(pv);
        sig(L, SY_KP + w * 4, s + 1, lane);
        if (s < 3) { stc(rs, l16, s == 0 ? pf0 : s == 1 ? pf1 : pf3, sent4); stc(rs, l16, s == 0 ? pf5 : s == 1 ? pf1 + 1024 : pf4, sent4); }
        if (s == 4) { VMW(0); publish_flag(SY_C1, OFF_F1 + b * 128, 3); }
        if (s == 4) { wait4(L, SY_KP, 1); load_wp(); }
      });
    }
    ST(4);
    __builtin_amdgcn_s_setprio(2);
    {
      unsigned hl = hilo(xsum(HS4(c4)));
      PIN(hl);
      v4u cr;
#pragma unroll 1
      for (unsigned sp = 0; sp < LSPIN_CAP; ++sp) {
        unsigned pv = *(volatile LAS unsigned*)(L + SY_VP + w * 4);
        cr = *(volatile LAS v4u*)(L + L_COR + w * 512 + r32 * 16);
        if ((unsigned)__builtin_amdgcn_readfirstlane(pv) >= 8u) break;
      }
      ST(29);
      v4u ca = {hh ? 0u : hl, hh ? 0u : hl, 0u, 0u}, cb = {hh ? 0u : cr.x, hh ? 0u : cr.y, 0u, 0u};
      acc = mfma32(ca, cb, acc);
      f32x16 ep, en;
      feature_map(acc, __uint_as_float(cr.z), wp, hh, ep, en, [&](auto) AI {});
      const int ro = L_PHI + (32 * w + r32) * 128, sw = r32 & 15;
#pragma unroll
      for (int g = 0; g < 4; ++g) {
        *(LAS v2u*)(L + ro + (((2 * g + h1) ^ sw) * 8)) = v2u{pk2(ep[4 * g], ep[4 * g + 1]), pk2(ep[4 * g + 2], ep[4 * g + 3])};
        *(LAS v2u*)(L + ro + (((8 + 2 * g + h1) ^ sw) * 8)) = v2u{pk2(en[4 * g], en[4 * g + 1]), pk2(en[4 * g + 2], en[4 * g + 3])};
      }
      sig(L, SY_KP + w * 4, 8, lane);
    }
    ST(5);
    int f1v;
    {
      const int i = lane & 15;
      const int tb1 = w == 0 ? i : ttB >> 2, hd1 = w == 0 ? hA : i;
      const int bA = ((tb1 >> 2) * 2 + (hd1 >> 3)) + 8 * ((tb1 & 3) * 8 + (hd1 & 7));
      const int tt2 = tbC * 4 + (i >> 2), bH = (tt2 >> 3) + 8 * ((tt2 & 7) * 4 + (i & 3));
      const int m = r32 < 28 ? r32 : 27, bD = (m & 7) + 8 * (7 * (m >> 3) + jj);
      f1v = (w < 2 ? bA : w == 2 ? bH : bD) * 128;
    }
    {
      const int r0 = 8 * g4 + q4, r1 = r0 + 4;
      const int ta0 = L_PHI + r0 * 128 + (((4 * w + p4) ^ (r0 & 15)) * 8), ta1 = L_PHI + r1 * 128 + (((4 * w + p4) ^ (r1 & 15)) * 8);
      const int tv = L_V + r0 * 72 + p4 * 8;
      wait8(L, 8, 7);
      WO_B(L_BK, 0, 7);
      wait8(L, 10, 8);
      ST(6);
      v2u fa0[4], fa1[4], b00[4], b01[4], b10[4], b11[4];
#pragma unroll
      for (int s = 0; s < 4; ++s) {
        fa0[s] = ldtr(L, ta0 + s * 4096); fa1[s] = ldtr(L, ta1 + s * 4096);
        b00[s] = ldtr(L, tv + s * 4096); b01[s] = ldtr(L, tv + s * 4096 + 288);
        b10[s] = ldtr(L, tv + s * 4096 + 32); b11[s] = ldtr(L, tv + s * 4096 + 288 + 32);
      }
      SCHED();
      f32x4 z0 = {}, z1 = {};
#pragma unroll
      for (int s = 0; s < 4; ++s) {
        const v4u af_ = {fa0[s].x, fa0[s].y, fa1[s].x, fa1[s].y};
        z0 = mfma16(af_, v4u{b00[s].x, b00[s].y, b01[s].x, b01[s].y}, z0);
        z1 = mfma16(af_, v4u{b10[s].x, b10[s].y, b11[s].x, b11[s].y}, z1);
      }
      auto s0 = __builtin_amdgcn_permlane32_swap(pk2(z0.x, z0.y), pk2(z1.x, z1.y), false, false);
      auto s1 = __builtin_amdgcn_permlane32_swap(pk2(z0.z, z0.w), pk2(z1.z, z1.w), false, false);
      stc(rs, slotB * 16, OFF_KVP + (hA * 16 + tbA) * 4096 + w * 1024, guard4(v4u{s0[0], s1[0], s0[1], s1[1]}));
      SCHED();
    }
    ST(7);
    __builtin_amdgcn_s_setprio(1);
    {
      v2u f1 = ldc2(rs, f1v, OFF_F1);
      flag_ensure(f1, rs, f1v, OFF_F1, tlo, thi);
    }
    sig(L, SY_SK + w * 4, 1, lane);
    WO_B(L_BK, 7, 14);
    wait4(L, SY_SK, 1);
    delay(KDLY);
    {
      v4u kp[16];
      const int ko = OFF_KVP + hA * 16 * 4096 + w * 1024;
      int kso[4];
#pragma unroll
      for (int g = 0; g < 4; ++g) kso[g] = ko + ((g + tbA) & 3) * 16384 - g * 16384;
      issue<0, 4, 4096>(kp, rs, l16, kso[0]); issue<4, 8, 4096>(kp, rs, l16, kso[1]); issue<8, 12, 4096>(kp, rs, l16, kso[2]); issue<12, 16, 4096>(kp, rs, l16, kso[3]);
      SCHED();
      sig(L, SY_KP + w * 4, 9, lane);
      ST(8);
      float sm[8] = {0.f, 0.f, 0.f, 0.f, 0.f, 0.f, 0.f, 0.f};
#define KGRP(g) do { ensure<4 * g, 4 * g + 4, 4096>(kp, rs, l16, kso[g]); _Pragma("unroll") for (int i = 4 * g; i < 4 * g + 4; ++i) ACC8(sm, kp[i]); \
      _Pragma("unroll") for (int j = 0; j < 8; ++j) PIN(sm[j]); } while (0)
      KGRP(0); LANDED(0); KGRP(1); KGRP(2); KGRP(3);
      *(LAS v4u*)(L + L_KV + w * 1024 + l16) = v4u{pk2(sm[0], sm[1]), pk2(sm[2], sm[3]), pk2(sm[4], sm[5]), pk2(sm[6], sm[7])};
      sig(L, SY_KP + w * 4, 10, lane);
    }
    __builtin_amdgcn_s_setprio(0);
    ST(9);
    rd_wo(L_BK);
    wait4(L, SY_SK, 3);
    WC_B(L_BK, 0, 14);
    XR_LOAD();
    wait4(L, SY_VP, 12);
    ST(10);
    delay(ODLY);
    __builtin_amdgcn_s_setprio(1);
    {
      v4u of[8];
      const int oo = OFF_O + ttB * 32768 + w * 8192;
      issue<0, 8, 1024>(of, rs, l16, oo);
      SCHED();
      ST(11);
      sig(L, SY_G + w * 4, 1, lane);
      VMW(8); LANDED(1);
      rd_wc(IC<0>{}, L_BK);
      ensure<0, 4, 1024>(of, rs, l16, oo);
#pragma unroll
      for (int i = 0; i < 4; ++i) *(LAS v4u*)(L + L_OIMG + w * 8192 + i * 1024 + l16) = of[i];
      ensure<4, 8, 1024>(of, rs, l16, oo);
#pragma unroll
      for (int i = 4; i < 8; ++i) *(LAS v4u*)(L + (w == 3 ? L_OIM2 - 4096 : L_OIMG + w * 8192) + i * 1024 + l16) = of[i];
      sig(L, SY_KP + w * 4, 11, lane);
    }
    PIN(xr[0]); PIN(xr[1]);
    phase_b2([&](auto I) AI { constexpr int i = decltype(I)::v; if constexpr (i % 2 == 0 && i / 2 < 14) WC1(L_BK, 14 + i / 2); });
    if (w == 3) {
      wait1(L, SY_S, 8);
      const int so = L_ST2 + r32 * 144 + h1 * 64;
      f32x4 p0 = *(LAS f32x4*)(L + so), p1 = *(LAS f32x4*)(L + so + 16), p2 = *(LAS f32x4*)(L + so + 32), p3 = *(LAS f32x4*)(L + so + 48);
      p0 += p1;
      if (cgB < 3) p0 += p2 + p3;
      float s1 = xsum(p0.x + p0.z), s2 = xsum(p0.y + p0.w);
      __builtin_amdgcn_raw_buffer_store_b64(v2u{guard(__float_as_uint(s1)), guard(__float_as_uint(s2))}, rs, hh ? OFF_END : OFF_HS + (ttB * 4 + cgB) * 256 + r32 * 8, 0, AUX_SC1);
    }
    if (b == 0 && tid == 0) *(GAS v2u*)(a.ws + OFF_EPOCH) = v2u{tlo, thi};
    if (actC) {
      delay(HDLY);
      v4u h0[7];
      issue<0, 7, 1024>(h0, rs, l16, hso);
      SCHED();
      sig(L, SY_G + w * 4, 2, lane);
      VMW(7); LANDED(2);
      rd_wc(IC<1>{}, L_BK);
      wait4(L, SY_SV, 5);
      h_stage(h0, 0);
      issue<0, 7, 1024>(hs, rs, l16, hso + 28672);
      q0 = ldc2(rs, hsv, hss); q1 = ldc2(rs, hsv, hss + 256);
      SCHED();
    }
    __builtin_amdgcn_s_setprio(0);
  } else {
    seg_issue(1);
    ST(27);
    v4u xf[28], phq[4];
    f32x4 a1 = {}, a2 = {};
    {
      const int row = 32 * t4 + r32, key = (row >> 1) & 7;
#pragma unroll
      for (int s = 0; s < 7; ++s) {
        VMW(s < 5 ? 5 : s == 5 ? 12 : 7);
        sig(L, SY_VP + t4 * 4, s + 1, lane);
        if (s == 0) ST(3);
        if (s == 3) ST(28);
#pragma unroll
        for (int i = 0; i < 4; ++i) xf[4 * s + i] = lds128(L, L_X + s * 16384 + row * 128 + (((2 * i + h1) ^ key) * 16));
        if (s < 5) seg_issue(s + 2);
        if (s == 4) {
          wait8(L, 3, 2);
#pragma unroll
          for (int i = 0; i < 7; ++i) dma(lb + L_WV + (4 * i + t4) * 1024, vk, rqkv, sk + 1792 + 64 * i * 2688);
        }
        SCHED();
#pragma unroll
        for (int i = 0; i < 4; ++i) { SUM4(a1, xf[4 * s + i]); SQ4(a2, xf[4 * s + i]); }
        PIN(a1); PIN(a2);
        SCHED();
      }
    }
    ST(4);
    __builtin_amdgcn_s_setprio(3);
    const float mean = xsum(HS4(a1)) * (1.f / DM), var = fmaxf(xsum(HS4(a2)) * (1.f / DM) - mean * mean, 0.f);
    const float rstd = __builtin_amdgcn_rsqf(var + 1e-5f);
    const unsigned hl = hilo(-mean);
    const unsigned mh = (hl & 0xffffu) * 0x10001u, ml = (hl >> 16) * 0x10001u;
    if (!hh) *(LAS v4u*)(L + L_COR + t4 * 512 + r32 * 16) = v4u{mh, ml, __float_as_uint(rstd), 0u};
    const v4u mfr = {hh ? 0u : mh, hh ? 0u : ml, 0u, 0u};
    sig(L, SY_VP + t4 * 4, 8, lane);
    ST(5);
    const int wtr = (8 * h1 + q4) * 64 + blk * 32 + p4 * 8 + ksh;
    auto cs_part = [&](int p, int base) AI {
      f32x4 c4 = {};
      v2u fl[7], fh[7];
#pragma unroll
      for (int i = 0; i < 7; ++i) { fl[i] = ldtr(L, base + wtr + (4 * i + t4) * 1024); fh[i] = ldtr(L, base + wtr + (4 * i + t4) * 1024 + 256); }
#pragma unroll
      for (int i = 0; i < 7; ++i) { const v4u f = {fl[i].x, fl[i].y, fh[i].x, fh[i].y}; SUM4(c4, f); }
      *(LAS float*)(L + L_V + t4 * 4096 + 2304 + p * 256 + lane * 4) = HS4(c4);
    };
    float cp[4];
    auto cs_read = [&](int p) AI {
#pragma unroll
      for (int i = 0; i < 4; ++i) cp[i] = *(LAS float*)(L + L_V + i * 4096 + 2304 + p * 256 + lane * 4);
    };
    auto cs_frag = [&]() AI -> v4u {
      unsigned x = hilo(xsum((cp[0] + cp[1]) + (cp[2] + cp[3])));
      x = hh ? 0u : x;
      return v4u{x, x, 0u, 0u};
    };
    wait8(L, 5, 5);
    VMW(0);
    cs_part(0, L_WV);
    sig(L, SY_VP + t4 * 4, 9, lane);
    wait4(L, SY_VP, 9);
    ST(6);
    cs_read(0);
    v4u csv;
    f32x16 acc = gemm_tr<28, 5>(xf, L, L_WV + wtr, [&](auto I) AI {
      constexpr int ks = decltype(I)::v;
      if (ks % 2 == 0 && ks / 2 < 7) dma(lb + L_WQ + (4 * (ks / 2) + t4) * 1024, vk, rqkv, sk + 64 * (ks / 2) * 2688);
      if (ks == 15) { csv = cs_frag(); PIN(csv); } });
    acc = mfma32(csv, mfr, acc);
    acc *= rstd;
    if (hh) { acc[12] = 1.0f; acc[13] = 0.f; acc[14] = 0.f; acc[15] = 0.f; }
    {
      const int ro = L_V + t4 * 4096 + r32 * 72 + h1 * 8;
#pragma unroll
      for (int g = 0; g < 4; ++g) *(LAS v2u*)(L + ro + g * 16) = v2u{pk2(acc[4 * g], acc[4 * g + 1]), pk2(acc[4 * g + 2], acc[4 * g + 3])};
    }
    sig(L, SY_VP + t4 * 4, 10, lane);
    __builtin_amdgcn_s_setprio(0);
    ST(7);
    load_wp();
    VMW(0);
    XR_LOAD();
    cs_part(1, L_WQ);
    sig(L, SY_VP + t4 * 4, 11, lane);
    wait4(L, SY_VP, 11);
    cs_read(1);
    acc = gemm_tr<28, 5>(xf, L, L_WQ + wtr, [&](auto I) AI { if (decltype(I)::v == 8) { csv = cs_frag(); PIN(csv); } });
    sig(L, SY_SV + t4 * 4, 1, lane);
    acc = mfma32(csv, mfr, acc);
    {
      f32x16 ep, en;
      feature_map(acc, rstd, wp, hh, ep, en, [&](auto) AI {});
      phq[0] = pack8(ep, 0); phq[1] = pack8(ep, 1); phq[2] = pack8(en, 0); phq[3] = pack8(en, 1);
      PIN(phq[0]); PIN(phq[1]); PIN(phq[2]); PIN(phq[3]);
    }
    ST(8);
    PIN(xr[0]); PIN(xr[1]);
    wait4(L, SY_KP, 9);
    wait4(L, SY_SV, 1);
    WO_B(L_BV, 0, VW1);
    __builtin_amdgcn_s_setprio(3);
    f32x16 o = {};
    {
      v4u kf[4];
#pragma unroll 1
      for (unsigned sp = 0; sp < LSPIN_CAP; ++sp) {
        v4u pv = *(volatile LAS v4u*)(L + SY_KP);
#pragma unroll
        for (int s = 0; s < 4; ++s) kf[s] = *(volatile LAS v4u*)(L + L_KV + s * 1024 + l16);
        if (min4u(pv) >= 10u) break;
        __builtin_amdgcn_s_sleep(1);
      }
      ST(9);
#pragma unroll
      for (int s = 0; s < 4; ++s) o = mfma32(kf[s], phq[s], o);
    }
    const float rd = __builtin_amdgcn_rcpf(upper(o[12]) + 1e-6f);
    o *= rd;
    v4u o0 = pack8(o, 0), o1 = pack8(o, 1);
    if (hh) { o1.z = 0; o1.w = 0; }
    const int oo = OFF_O + ((tbA * 4 + t4) * 16 + hA) * 2048;
    stc(rs, l16, oo, guard4(o0)); stc(rs, l16, oo + 1024, guard4(o1));
    SCHED();
    __builtin_amdgcn_s_setprio(0);
    sig(L, SY_VP + t4 * 4, 12, lane);
    ST(10);
    WO_B(L_BV, VW1, 14);
    VMW(0); LANDED(0);
    rd_wo(L_BV);
    wait4(L, SY_SV, 3);
    wait4(L, SY_G, 1);
    WC_B(L_BV, 0, 14);
    ST(11);
    phase_b2([&](auto) AI {});
    if (actC) {
      VMW(1); LANDED(1);
      rd_wc(IC<0>{}, L_BV);
      wait4(L, SY_SV, 5);
      wait4(L, SY_G, 2);
      WC_B(L_BV2, 14, 28);
      v4u h2[7];
      issue<0, 7, 1024>(h2, rs, l16, hso);
      issue<0, 7, 1024>(hs, rs, l16, hso + 28672);
      SCHED();
      VMW(14); LANDED(2);
      rd_wc(IC<1>{}, L_BV2);
      wait4(L, SY_SV, 7);
      h_stage(h2, 0);
    }
  }
  if (actC) {
    ST(14);
    { unsigned hl = hilo(xsum(HS4(c4c))); hl = hh ? 0u : hl; csf = v4u{hl, hl, 0u, 0u}; PIN(csf); }
    const int ao = OFF_ACT + (tbC * 4 * 112 + TC) * 1024;
    wait1(L, SY_H, 4);
    ST(15);
    f32x16 accA, accB;
    accA = gemm_hook<28, 6, true>(wc, L, L_H + l16, f32x16{}, [&](auto I) AI {
      constexpr int ks = decltype(I)::v;
      if (ks % 6 == 0 && ks / 6 < 4) stc(rs, l16, ao + (ks / 6) * 114688, sent4);
      (void)ks;
    });
    if (w < 4) {
#pragma unroll 1
      for (unsigned sp = 0; sp < SPIN_CAP; ++sp) {
        if (__builtin_expect(!__any(max(max(q0.x, q0.y), max(q1.x, q1.y)) == SENT), 1)) break;
        __builtin_amdgcn_s_sleep(RSLP);
        CFENCE();
        q0 = ldc2(rs, hsv, hss); q1 = ldc2(rs, hsv, hss + 256);
      }
      float mean2 = xsum(__uint_as_float(q0.x) + __uint_as_float(q1.x)) * (1.f / DM);
      float var2 = fmaxf(xsum(__uint_as_float(q0.y) + __uint_as_float(q1.y)) * (1.f / DM) - mean2 * mean2, 0.f) + 1e-5f;
      unsigned hl2 = hilo(-mean2);
      if (!hh) *(LAS v4u*)(L + L_COR + t4 * 512 + r32 * 16) = v4u{(hl2 & 0xffffu) * 0x10001u, (hl2 >> 16) * 0x10001u, __float_as_uint(__builtin_amdgcn_rsqf(var2)), __float_as_uint(var2)};
    }
    h_stage(hs, 1);
    ST(16);
    v4u wd[28];
    int rot[7];
#pragma unroll
    for (int p = 0; p < 7; ++p) { int r_ = p + jj; rot[p] = (r_ >= 7 ? r_ - 7 : r_) * 2048 + 14 * w * 1024; }
    const int wdo = OFF_WD + 2 * jj * 112 * 1024;
#define WD_OFF(n) (wdo + rot[((n) % 14) >> 1] + ((n) & 1) * 1024 + ((n) >= 14 ? 114688 : 0))
    constexpr int NPRE = 11, NLDS = 4;
    wait1(L, SY_H + 4, 4);
    ST(17);
    const int tE = eC ? 1 : 0, tL = eC ? 2 : 3;
    const int rt = tid >> 8, tok = (tid >> 3) & 31, c8 = tid & 7;
    float k1, k2, vr, tz[8], tn[8]; unsigned ow[4];
    v4u cr = lds128(L, L_COR + r32 * 16), crn = lds128(L, L_COR + 512 + r32 * 16);
    v4u buf[4];
#pragma unroll
    for (int i = 0; i < 4; ++i) buf[i] = lds128(L, HB(1) + i * 1024 + l16);
    accA = mfma32(csf, v4u{hh ? 0u : cr.x, hh ? 0u : cr.y, 0u, 0u}, accA);
    auto epi_consts = [&]() AI {
      const float rr = __uint_as_float(cr.z);
      vr = __uint_as_float(cr.w);
      k1 = (-2.f * 0.7978845608028654f * L2E) * rr; k2 = k1 * 0.044715f * rr * rr;
    };
    auto epi_step = [&](const f32x16& pv, int s) AI {
      const int e = s / 3, ph = s % 3;
      if (ph == 0) { float g = pv[e]; tz[e] = __builtin_fmaf(k2, g * g, k1) * g; PIN(tz[e]); }
      else if (ph == 1) { tz[e] = __builtin_amdgcn_exp2f(tz[e]); tn[e] = pv[e] * pv[e + 8]; tz[e] = __builtin_fmaf(tz[e], vr, vr); PIN(tz[e]); PIN(tn[e]); }
      else { tn[e] *= __builtin_amdgcn_rcpf(tz[e]); if (e & 1) { ow[e >> 1] = guard(pk2(tn[e - 1], tn[e])); PIN(ow[e >> 1]); } else PIN(tn[e]); }
    };
    int f3v; { int j = (14 * w) / 8 + (lane < 2 ? lane : 2); j = j < 14 ? j : 13; f3v = (tbC * 16 + j) * 128; }
    v2u f3, hr0, hr1;
#define C_TILE(t, CUR, PRV) do { \
      epi_consts(); \
      CUR = mfma32(csf, v4u{hh ? 0u : crn.x, hh ? 0u : crn.y, 0u, 0u}, f32x16{}); \
      cr = crn; \
      if ((t) < 3) crn = lds128(L, L_COR + ((t) + 1) * 512 + r32 * 16); \
      SCHED(); \
      _Pragma("unroll") for (int ks = 0; ks < 28; ++ks) { \
        CUR = mfma32(wc[ks], buf[ks % 4], CUR); \
        if ((t) == 1 && ks == 2) { PIN(CUR); F3_PUB(); } \
        if ((t) < 3 && ks == 23) { PIN(CUR); wait1(L, SY_H + ((t) + 1) * 4, 4); } \
        if ((t) < 3 || ks + 4 < 28) buf[ks % 4] = lds128(L, (ks + 4 < 28 ? HB(t) + (ks + 4) * 1024 : HB((t) + 1) + (ks - 24) * 1024) + l16); \
        if (ks >= 2 && ks < 26) epi_step(PRV, ks - 2); \
        if (ks == 26) stc(rs, l16, ao + ((t) - 1) * 114688, v4u{ow[0], ow[1], ow[2], ow[3]}); \
        if ((t) == 1 && ks >= 4 && (ks % 4) == 0) wd[ks / 4 - 1] = ldc(rs, l16, WD_OFF(ks / 4 - 1)); \
        if ((t) == 2 && (ks % 3) == 0 && 6 + ks / 3 < NPRE) wd[6 + ks / 3] = ldc(rs, l16, WD_OFF(6 + ks / 3)); \
        if ((t) == 2 && (ks % 3) == 0 && 6 + ks / 3 >= NPRE && 6 + ks / 3 - NPRE < NLDS) dma_c(lb + L_WDL + (w * NLDS + 6 + ks / 3 - NPRE) * 1024, l16, rs, WD_OFF(28 - NLDS + 6 + ks / 3 - NPRE)); \
        if ((t) == 3 && ks == 0) { f3 = ldc2(rs, f3v, OFF_F3); \
          int sl = 4 * jj + (c8 >> 1) - 7 * rC; sl = sl < 0 ? sl + 28 : sl; \
          const int o_ = HB(rt ? tL : tE) + sl * 1024 + tok * 16 + (c8 & 1) * 8; \
          hr0 = *(LAS v2u*)(L + o_); hr1 = *(LAS v2u*)(L + o_ + 512); } \
        if ((t) == 3 && (ks & 1) == 1 && NPRE + (ks >> 1) < 28 - NLDS) wd[NPRE + (ks >> 1)] = ldc(rs, l16, WD_OFF(NPRE + (ks >> 1))); \
        SCHED(); \
      } } while (0)
#define F3_PUB() do { VMW(0); publish_flag(SY_C3, OFF_F3 + (tbC * 16 + 7 * eC + jj) * 128, 7); } while (0)
    if (w >= 4) __builtin_amdgcn_s_setprio(CPRIO);
    C_TILE(1, accB, accA);
    C_TILE(2, accA, accB);
    ST(18);
    C_TILE(3, accB, accA);
    ST(19);
    epi_consts();
#pragma unroll
    for (int s = 0; s < 24; ++s) epi_step(accB, (s % 8) * 3 + s / 8);
    stc(rs, l16, ao + 3 * 114688, v4u{ow[0], ow[1], ow[2], ow[3]});
    SCHED();
    ST(20);
    flag_ensure(f3, rs, f3v, OFF_F3, tlo, thi);
    v4u af[14];
    const int aoE = OFF_ACT + (tbC * 4 + tE) * 114688, aoL = OFF_ACT + (tbC * 4 + tL) * 114688;
#define AF_OFF(n) (rot[(n) >> 1] + ((n) & 1) * 1024)
#define AF_ENSURE(LO, HI, BASE) do { unsigned m = 0; \
      _Pragma("unroll") for (int n = LO; n < HI; ++n) m = mx4(m, af[n]); \
      if (__builtin_expect(__any(m == SENT), 0)) { \
        _Pragma("unroll 1") for (unsigned sp = 0; sp < SPIN_CAP; ++sp) { \
          __builtin_amdgcn_s_sleep(RSLP); CFENCE(); m = 0; \
          _Pragma("unroll") for (int n = LO; n < HI; ++n) af[n] = ldc(rs, l16, (BASE) + AF_OFF(n)); \
          _Pragma("unroll") for (int n = LO; n < HI; ++n) m = mx4(m, af[n]); \
          if (!__any(m == SENT)) break; } } } while (0)
#pragma unroll
    for (int n = 0; n < 14; ++n) af[n] = ldc(rs, l16, aoE + AF_OFF(n));
    SCHED();
    VMW(15);
#pragma unroll
    for (int n = 0; n < NLDS; ++n) wd[28 - NLDS + n] = lds128(L, L_WDL + (w * NLDS + n) * 1024 + l16);
    arrive(L, SY_C2, lane);
    ST(21);
    auto red_out = [&](int ph, const f32x16& d0, const f32x16& d1) AI {
      const int ro = L_RED + (ph * 8 + w) * 4608 + r32 * 144 + h1 * 8;
#pragma unroll
      for (int g = 0; g < 4; ++g) {
        *(LAS v2u*)(L + ro + g * 16) = v2u{pk2(d0[4 * g], d0[4 * g + 1]), pk2(d0[4 * g + 2], d0[4 * g + 3])};
        *(LAS v2u*)(L + ro + 64 + g * 16) = v2u{pk2(d1[4 * g], d1[4 * g + 1]), pk2(d1[4 * g + 2], d1[4 * g + 3])};
      }
    };
    {
      f32x16 d0 = {}, d1 = {};
      AF_ENSURE(0, 6, aoE);
#pragma unroll
      for (int n = 0; n < 6; ++n) { d0 = mfma32(wd[n], af[n], d0); d1 = mfma32(wd[14 + n], af[n], d1); SCHED(); }
      PIN(d0); PIN(d1);
      AF_ENSURE(6, 14, aoE);
      wait1(L, SY_C2, 8);
#pragma unroll
      for (int n = 0; n < 6; ++n) af[n] = ldc(rs, l16, aoL + AF_OFF(n));
      SCHED();
#pragma unroll
      for (int n = 6; n < 14; ++n) { d0 = mfma32(wd[n], af[n], d0); d1 = mfma32(wd[14 + n], af[n], d1); af[n] = ldc(rs, l16, aoL + AF_OFF(n)); SCHED(); }
      red_out(0, d0, d1);
    }
    ST(22);
    {
      f32x16 d0 = {}, d1 = {};
      AF_ENSURE(0, 6, aoL);
#pragma unroll
      for (int n = 0; n < 6; ++n) { d0 = mfma32(wd[n], af[n], d0); d1 = mfma32(wd[14 + n], af[n], d1); SCHED(); }
      PIN(d0); PIN(d1);
      AF_ENSURE(6, 14, aoL);
      ST(23);
#pragma unroll
      for (int n = 6; n < 14; ++n) { d0 = mfma32(wd[n], af[n], d0); d1 = mfma32(wd[14 + n], af[n], d1); }
      red_out(1, d0, d1);
    }
    ST(24);
    bar();
    {
      float sm[8] = {lo_f(hr0.x), hi_f(hr0.x), lo_f(hr0.y), hi_f(hr0.y), lo_f(hr1.x), hi_f(hr1.x), lo_f(hr1.y), hi_f(hr1.y)};
      v4u v[8];
#pragma unroll
      for (int p = 0; p < 8; ++p) v[p] = lds128(L, L_RED + (rt * 8 + p) * 4608 + tok * 144 + c8 * 16);
#pragma unroll
      for (int p = 0; p < 8; ++p) ACC8(sm, v[p]);
      u16* op = a.out + (size_t)((tbC * 4 + (rt ? tL : tE)) * 32 + tok) * DM + 64 * jj + 8 * c8;
      *(GAS v4u*)op = v4u{pk2(sm[0], sm[1]), pk2(sm[2], sm[3]), pk2(sm[4], sm[5]), pk2(sm[6], sm[7])};
    }
  }
  asm volatile("hk_code_end:");
}
}

extern "C" void kernel_launch(void* const* d_in, const int* in_sizes, int n_in, void* d_out, int out_size, void* d_ws, size_t ws_size, hipStream_t stream) {
  using namespace hk;
  Args a;
  a.x = (const u16*)d_in[0]; a.wqkv = (const u16*)d_in[1]; a.wphi = (const u16*)d_in[2]; a.wo = (const u16*)d_in[3];
  a.wg = (const u16*)d_in[5]; a.wu = (const u16*)d_in[6]; a.wd = (const u16*)d_in[7];
  a.out = (u16*)d_out; a.ws = (char*)d_ws;
  (void)hipFuncSetAttribute((const void*)k_main, hipFuncAttributeMaxDynamicSharedMemorySize, LDS_BYTES);
  k_main<<<256, 512, LDS_BYTES, stream>>>(a);
}
```

```cpp
#include <hip/hip_runtime.h>

namespace hk {
typedef unsigned short u16;
typedef unsigned long long u64;
typedef unsigned v4u __attribute__((ext_vector_type(4)));
typedef unsigned v2u __attribute__((ext_vector_type(2)));
typedef float f32x16 __attribute__((ext_vector_type(16)));
typedef float f32x4 __attribute__((ext_vector_type(4)));
typedef float f32x2 __attribute__((ext_vector_type(2)));
typedef __bf16 bf16x8 __attribute__((ext_vector_type(8)));
typedef __bf16 bf16x2 __attribute__((ext_vector_type(2)));
typedef short s16x4 __attribute__((ext_vector_type(4)));
typedef __amdgpu_buffer_rsrc_t rsrc_t;
#define GAS __attribute__((address_space(1)))
#define LAS __attribute__((address_space(3)))
#define DI __device__ __forceinline__
#define AI __attribute__((always_inline))
#define __uint_as_float(u) __builtin_bit_cast(float, (unsigned)(u))
#define __float_as_uint(f) __builtin_bit_cast(unsigned, (float)(f))
#define __any(p) (__builtin_amdgcn_ballot_w64(p) != 0ull)
#define SCHED() __builtin_amdgcn_sched_barrier(0)
#define CFENCE() asm volatile("" ::: "memory")
#define VMW(n) asm volatile("s_waitcnt vmcnt(%0)" ::"i"(n) : "memory")
#define PIN(x) asm volatile("" : "+v"(x))
#define RSRC(p, n) __builtin_amdgcn_make_buffer_rsrc((void*)(p), 0, (n), 0x00020000)

#define RSLP 2
#define FSLP 4
constexpr int SEQ = 2048, DM = 448, DFF = 1792;
constexpr int OFF_EPOCH = 0, OFF_F1 = 32768, OFF_F2 = 65536, OFF_FD = 98304, OFF_F3 = 131072;
constexpr int OFF_WD = 163840;
constexpr int OFF_KVP = OFF_WD + 14 * 112 * 1024;
constexpr int OFF_O = OFF_KVP + 16 * 16 * 4096;
constexpr int OFF_H = OFF_O + 64 * 16 * 2048;
constexpr int OFF_ACT = OFF_H + 64 * 28 * 1024;
constexpr int OFF_HS = OFF_ACT + 64 * 112 * 1024;
constexpr int OFF_END = OFF_HS + 64 * 1024;
constexpr unsigned SENT = 0xFFFFFFFFu;
constexpr unsigned SPIN_CAP = 1u << 15, LSPIN_CAP = 1u << 21;
constexpr int AUX_SC1 = 16;

constexpr int L_X = 0;
constexpr int L_WK = 114688;
constexpr int L_WPHI = 159744;
constexpr int L_JUNK = L_WPHI + 1600;
constexpr int L_SY = 161600;
constexpr int L_COR = 161792;
constexpr int L_WV = 0, L_WQ = 28672;
constexpr int L_PHI = 143360;
constexpr int L_V = 4 * 16384;
constexpr int L_KV = 5 * 16384;
constexpr int L_BK = 86016, L_BV = 0;
constexpr int L_OK = 57344, L_OV = 143360;
constexpr int L_RS = 57344;
constexpr int L_ST2 = 73728;
constexpr int L_COR2 = 83968;
constexpr int L_RED = 0;
constexpr int LDS_BYTES = 163840;
constexpr int C_H = 0  , C_D = 16, C_B = 20, C_S = 24, C_R = 28, C_E = 32, C_F1 = 36, C_F2 = 40, C_FD = 44, SY_C3 = L_SY + 160;

DI unsigned pk2(float a, float b) { f32x2 v = {a, b}; return __builtin_bit_cast(unsigned, __builtin_convertvector(v, bf16x2)); }
DI float lo_f(unsigned u) { return __uint_as_float(u << 16); }
DI float hi_f(unsigned u) { return __uint_as_float(u & 0xffff0000u); }
DI float dot2(unsigned a, unsigned b, float c) { return __builtin_amdgcn_fdot2_f32_bf16(__builtin_bit_cast(bf16x2, a), __builtin_bit_cast(bf16x2, b), c, false); }
DI f32x16 mfma32(v4u a, v4u b, f32x16 c) { return __builtin_amdgcn_mfma_f32_32x32x16_bf16(__builtin_bit_cast(bf16x8, a), __builtin_bit_cast(bf16x8, b), c, 0, 0, 0); }
DI f32x4 mfma16(v4u a, v4u b, f32x4 c) { return __builtin_amdgcn_mfma_f32_16x16x32_bf16(__builtin_bit_cast(bf16x8, a), __builtin_bit_cast(bf16x8, b), c, 0, 0, 0); }
DI void bar() { asm volatile("s_waitcnt lgkmcnt(0)" ::: "memory"); __builtin_amdgcn_s_barrier(); CFENCE(); }
DI float xsum(float v) { unsigned u = __float_as_uint(v); auto r = __builtin_amdgcn_permlane32_swap(u, u, false, false); return __uint_as_float(r[0]) + __uint_as_float(r[1]); }
DI float xmax(float v) { unsigned u = __float_as_uint(v); auto r = __builtin_amdgcn_permlane32_swap(u, u, false, false); return fmaxf(__uint_as_float(r[0]), __uint_as_float(r[1])); }
DI float xmin(float v) { unsigned u = __float_as_uint(v); auto r = __builtin_amdgcn_permlane32_swap(u, u, false, false); return fminf(__uint_as_float(r[0]), __uint_as_float(r[1])); }
DI float upper(float v) { unsigned u = __float_as_uint(v); auto r = __builtin_amdgcn_permlane32_swap(u, u, false, false); return __uint_as_float(r[1]); }
DI unsigned guard(unsigned u) { return u < 0xFFFFFFFEu ? u : 0xFFFFFFFEu; }
DI v4u guard4(v4u v) { return v4u{guard(v.x), v.y, guard(v.z), v.w}; }
DI unsigned mx4(unsigned m, v4u a) { return max(m, max(a.x, a.z)); }
DI v4u ldc(rsrc_t rs, int voff, int soff) { return __builtin_amdgcn_raw_buffer_load_b128(rs, voff, soff, AUX_SC1); }
DI v2u ldc2(rsrc_t rs, int voff, int soff) { return __builtin_amdgcn_raw_buffer_load_b64(rs, voff, soff, AUX_SC1); }
DI void stc(rsrc_t rs, int voff, int soff, v4u v) { __builtin_amdgcn_raw_buffer_store_b128(v, rs, voff + soff, 0, AUX_SC1); }
DI v4u lds128(LAS char* L, int off) { return *(LAS v4u*)(L + off); }
DI v2u ldtr(LAS char* L, int off) { return __builtin_bit_cast(v2u, __builtin_amdgcn_ds_read_tr16_b64_v4i16((LAS s16x4*)(L + off))); }
DI v4u pack8(const f32x16& a, int s) { return v4u{pk2(a[8 * s], a[8 * s + 1]), pk2(a[8 * s + 2], a[8 * s + 3]), pk2(a[8 * s + 4], a[8 * s + 5]), pk2(a[8 * s + 6], a[8 * s + 7])}; }
#define DMA(lbase, LC, voff, r, sbase, GC) do { int t_; \
  asm volatile("s_add_u32 m0, %1, %5\n\ts_add_u32 %0, %4, %6\n\tbuffer_load_dwordx4 %2, %3, %0 offen lds" : "=&s"(t_) : "s"(lbase), "v"(voff), "s"(r), "s"(sbase), "i"(LC), "i"(GC) : "memory", "scc"); } while (0)
#define DMAC(lbase, LC, voff, r, sbase, GC) do { int t_; \
  asm volatile("s_add_u32 m0, %1, %5\n\ts_add_u32 %0, %4, %6\n\tbuffer_load_dwordx4 %2, %3, %0 offen sc1 lds" : "=&s"(t_) : "s"(lbase), "v"(voff), "s"(r), "s"(sbase), "i"(LC), "i"(GC) : "memory", "scc"); } while (0)
DI void dma(int lds, int voff, rsrc_t r, int soff) {
  asm volatile("s_mov_b32 m0, %0\n\ts_nop 0\n\tbuffer_load_dwordx4 %1, %2, %3 offen lds" ::"s"(lds), "v"(voff), "s"(r), "s"(soff) : "memory");
}
DI void touch(int lds, int voff, rsrc_t r, int soff) {
  asm volatile("s_mov_b32 m0, %0\n\ts_nop 0\n\tbuffer_load_dword %1, %2, %3 offen lds" ::"s"(lds), "v"(voff), "s"(r), "s"(soff) : "memory");
}
#define LDA4(d, voff, r, soff, IMM) asm volatile("buffer_load_dwordx4 %0, %1, %2, %3 offen offset:%4 sc1" : "=v"(d) : "v"(voff), "s"(r), "s"(soff), "i"(IMM) : "memory")
#define LDA2(d, voff, r, soff) asm volatile("buffer_load_dwordx2 %0, %1, %2, %3 offen sc1" : "=v"(d) : "v"(voff), "s"(r), "s"(soff) : "memory")
template <int LO, int HI, int N> DI void issue_a(v4u (&a)[N], rsrc_t rs, int voff, int soff) {
#pragma unroll
  for (int i = LO; i < HI; ++i) LDA4(a[i], voff, rs, soff + (i >> 2) * 4096, (i & 3) * 1024);
}
template <int LO, int HI, int N> DI void pin_all(v4u (&a)[N]) {
#pragma unroll
  for (int i = LO; i < HI; ++i) PIN(a[i]);
}
template <int LO, int HI, int N> DI bool missing(v4u (&a)[N]) {
  unsigned m = max(a[LO].x, a[LO].z);
#pragma unroll
  for (int i = LO + 1; i < HI; ++i) m = mx4(m, a[i]);
  return __any(m == SENT);
}
template <int LO, int HI, int N> DI void ensure(v4u (&a)[N], rsrc_t rs, int voff, int soff) {
  if (__builtin_expect(missing<LO, HI>(a), 0)) {
#pragma unroll 1
    for (unsigned sp = 0; sp < SPIN_CAP; ++sp) {
      __builtin_amdgcn_s_sleep(RSLP);
      issue_a<LO, HI>(a, rs, voff, soff);
      VMW(0); pin_all<LO, HI>(a);
      if (!missing<LO, HI>(a)) break;
    }
  }
}
DI void flag_ensure(v2u f, rsrc_t rs, int voff, int soff, unsigned tlo, unsigned thi) {
  if (__builtin_expect(__any(((f.x ^ tlo) | (f.y ^ thi)) != 0), 0)) {
#pragma unroll 1
    for (unsigned sp = 0; sp < SPIN_CAP; ++sp) {
      __builtin_amdgcn_s_sleep(FSLP);
      CFENCE();
      f = ldc2(rs, voff, soff);
      if (!__any(((f.x ^ tlo) | (f.y ^ thi)) != 0)) break;
    }
  }
  CFENCE();
}
#define INC(addr, OFF) do { const int a_ = (addr); const unsigned o1_ = one; asm volatile("ds_add_u32 %0, %1 offset:%2" ::"v"(a_), "v"(o1_), "i"(OFF) : "memory"); } while (0)
#define WAIT_BODY(RD, PRE, TV) RD "\n\t" PRE "s_mov_b32 %1, 0x200000\n" \
  "1:\n\ts_waitcnt lgkmcnt(0)\n\tv_cmp_gt_u32 vcc, " TV ", %0\n\ts_cbranch_vccz 2f\n\ts_sleep 1\n\t" RD "\n\ts_sub_u32 %1, %1, 1\n\ts_cbranch_scc0 1b\n\ts_waitcnt lgkmcnt(0)\n2:"
#define WAITVK(VT, KT) do { unsigned t_, c_, tv_; \
  asm volatile(WAIT_BODY("ds_read_b32 %0, %3", "v_cndmask_b32_e64 %2, %4, %5, %6\n\t", "%2") : "=&v"(t_), "=&s"(c_), "=&v"(tv_) : "v"(r8), "i"(VT), "i"(KT), "s"(0xF0F0F0F0F0F0F0F0ull) : "memory", "vcc", "scc"); } while (0)
#define WAITC(addr, OFF, T) do { unsigned t_, c_; \
  asm volatile(WAIT_BODY("ds_read_b32 %0, %2 offset:%3", "", "%4") : "=&v"(t_), "=&s"(c_) : "v"(addr), "i"(OFF), "i"(T) : "memory", "vcc", "scc"); } while (0)
DI void delay(int n) { for (int i = 0; i < n; ++i) __builtin_amdgcn_s_sleep(1); }

template <int V> struct IC { static constexpr int v = V; };
template <int I, int N, class F> DI void sfor(F&& f) { if constexpr (I < N) { f(IC<I>{}); sfor<I + 1, N>(f); } }

#define ST(k)

struct Args { const u16 *x, *wqkv, *wphi, *wo, *wg, *wu, *wd; u16* out; char* ws; };
typedef const __attribute__((address_space(4))) Args* KA;

constexpr float L2E = 1.4426950408889634f;
#define SUM4(a, v) do { a.x = dot2(v.x, ONES, a.x); a.y = dot2(v.y, ONES, a.y); a.z = dot2(v.z, ONES, a.z); a.w = dot2(v.w, ONES, a.w); } while (0)
#define SQ4(a, v) do { a.x = dot2(v.x, v.x, a.x); a.y = dot2(v.y, v.y, a.y); a.z = dot2(v.z, v.z, a.z); a.w = dot2(v.w, v.w, a.w); } while (0)
#define HS4(a) ((a.x + a.y) + (a.z + a.w))
#define ACC8(sm, v) do { sm[0] = dot2(v.x, SEL_LO, sm[0]); sm[1] = dot2(v.x, SEL_HI, sm[1]); sm[2] = dot2(v.y, SEL_LO, sm[2]); sm[3] = dot2(v.y, SEL_HI, sm[3]); \
    sm[4] = dot2(v.z, SEL_LO, sm[4]); sm[5] = dot2(v.z, SEL_HI, sm[5]); sm[6] = dot2(v.w, SEL_LO, sm[6]); sm[7] = dot2(v.w, SEL_HI, sm[7]); } while (0)
DI unsigned hilo(float v) { unsigned h = pk2(v, 0.f) & 0xffffu; float r = v - lo_f(h); return h | (pk2(r, 0.f) << 16); }

DI void feature_map(const f32x16& acc, float rstd, const v4u (&wp)[2], bool hh, f32x16& ep, f32x16& en) {
  f32x16 t = acc * rstd;
  v4u t0 = pack8(t, 0), t1 = pack8(t, 1);
  if (hh) { t1.z = 0; t1.w = 0; }
  f32x16 p = {};
  p = mfma32(wp[0], t0, p);
  p = mfma32(wp[1], t1, p);
  SCHED();
  float mx = fmaxf(fmaxf(p[12], p[13]), fmaxf(p[14], p[15])), mn = fminf(fminf(p[12], p[13]), fminf(p[14], p[15]));
  mx = hh ? -__builtin_inff() : mx; mn = hh ? __builtin_inff() : mn;
#pragma unroll
  for (int i = 0; i < 12; i += 2) { mx = fmaxf(fmaxf(mx, p[i]), p[i + 1]); mn = fminf(fminf(mn, p[i]), p[i + 1]); }
  mx = xmax(mx); mn = xmin(mn);
  SCHED();
  float bp = -mx * L2E, bn = mn * L2E;
  float bpp = hh ? -__builtin_inff() : bp, bnp = hh ? -__builtin_inff() : bn;
  float sp0 = 0.f, sp1 = 0.f, sn0 = 0.f, sn1 = 0.f;
#pragma unroll
  for (int q = 0; q < 4; ++q) {
#pragma unroll
    for (int i = 4 * q; i < 4 * q + 4; ++i) {
      ep[i] = __builtin_amdgcn_exp2f(__builtin_fmaf(p[i], L2E, i < 12 ? bp : bpp));
      en[i] = __builtin_amdgcn_exp2f(__builtin_fmaf(p[i], -L2E, i < 12 ? bn : bnp));
    }
    if (q > 0) {
      sp0 += ep[4 * q - 4] + ep[4 * q - 3]; sp1 += ep[4 * q - 2] + ep[4 * q - 1]; sn0 += en[4 * q - 4] + en[4 * q - 3]; sn1 += en[4 * q - 2] + en[4 * q - 1];
    }
    SCHED();
  }
  sp0 += ep[12] + ep[13]; sp1 += ep[14] + ep[15]; sn0 += en[12] + en[13]; sn1 += en[14] + en[15];
  float rp = __builtin_amdgcn_rcpf(xsum(sp0 + sp1)), rn = __builtin_amdgcn_rcpf(xsum(sn0 + sn1));
  ep *= rp;
  en *= rn;
}

#define NKV 14
#define PFS 0
#define THR 8
#define VGP 3
#define QGP 2
#define KDLY 2
#define ODLY 2
#define HDLY 0
#define CPRIO 1
#define NOB 6
#define ROT 0
#define RB0 (ROT ? rC : 0)
#if defined(__HIP_DEVICE_COMPILE__)
#define NOPK __attribute__((target("no-packed-fp32-ops")))
#else
#define NOPK
#endif
#define WV1 16
#define WCV 0
#define WCK 9
#define TWC 1
#define HSL 1
#define HCS 1
#define NW3 6
#define NBF 4
#define WVP 1
#define WQP 5
#define KO1 0
#define QV0 0
#define OW 1
#define CSV 1
__global__ void __launch_bounds__(512, 2) NOPK k_main(Args a) {
  extern __shared__ __attribute__((aligned(16))) char smem[];
  LAS char* L = (LAS char*)smem;
  const int lb = (int)(unsigned)(size_t)L;
  const int b = blockIdx.x, xcd = b & 7, ci = b >> 3;
  const int tid = threadIdx.x, lane = tid & 63, w = __builtin_amdgcn_readfirstlane(tid >> 6);
  ST(1);
  if (w == 7) {
    u64 pc; unsigned len;
    asm volatile("s_getpc_b64 %0\nhk_code_begin:\n\ts_mov_b32 %1, hk_code_end-hk_code_begin" : "=s"(pc), "=s"(len));
    touch(lb + L_JUNK, ((ci & 7) * 64 + lane) * 128, RSRC(pc, len), 0);
  }
  if (w == 3 && lane < 48) *(LAS unsigned*)(L + L_SY + lane * 4) = 0u;
  const rsrc_t rqkv = RSRC(a.wqkv, DM * 3 * DM * 2), rx = RSRC(a.x, SEQ * DM * 2);
  const int t4 = w & 3, G = w >> 2;
  const int tbA = 4 * (xcd >> 1) + (ci >> 3), hA = 8 * (xcd & 1) + (ci & 7);
  const int ksh = hA == 15 ? 8 : 0;
  const int xr_ = lane >> 3;
  const int ve = xr_ * 896 + (((lane & 7) ^ (xr_ >> 1)) * 16), vod = ve ^ 64;
  const int vk = (lane >> 2) * 2688 + (lane & 3) * 16;
  const int sx = (tbA * 128 + 32 * t4) * 896, sk = 16 * t4 * 2688 + 56 * hA - ksh;
  const int l1k = lb + t4 * 1024, l4k = lb + t4 * 4096;
#define SEG_ISSUE(s) do { DMA(l1k, L_WK + (s) * 4096, vk, rqkv, sk, 896 + 64 * (s) * 2688); \
    DMA(l4k, L_X + (s) * 16384, ve, rx, sx, (s) * 128); DMA(l4k, L_X + (s) * 16384 + 1024, vod, rx, sx, 8 * 896 + (s) * 128); \
    DMA(l4k, L_X + (s) * 16384 + 2048, ve, rx, sx, 16 * 896 + (s) * 128); DMA(l4k, L_X + (s) * 16384 + 3072, vod, rx, sx, 24 * 896 + (s) * 128); } while (0)
  ST(25);
  if (w < 2) { if (lane < 56) touch(lb + L_JUNK, (ci * 112 + w * 56 + lane) * 128, rx, (xcd >> 1) * 512 * 896); }
  else if (w < 4) { const int j = ci * 56 + lane; if (lane < 56) touch(lb + L_JUNK, (j >> 2) * 2688 + (j & 3) * 128, rqkv, (7 * (w - 1) + 3 * (xcd & 1)) * 128); }
  else SEG_ISSUE(0);
  ST(26);
  bar();
  int lane_ = lane, b_ = b;
  asm volatile("" : "+v"(lane_), "+s"(b_));
#define lane lane_
  const int r32 = lane & 31; const bool hh = lane >= 32; const int h1 = lane >> 5;
  const int q4 = (lane & 15) >> 2, p4 = lane & 3, blk = (lane >> 4) & 1, g4 = lane >> 4, c16 = lane & 15;
  const rsrc_t rs = RSRC(a.ws, OFF_END);
  const int l16 = lane * 16;
  unsigned ONES, SEL_LO, SEL_HI;
  asm volatile("s_mov_b32 %0, 0x3f803f80\n\ts_mov_b32 %1, 0x3f80\n\ts_mov_b32 %2, 0x3f800000" : "=s"(ONES), "=s"(SEL_LO), "=s"(SEL_HI));
  const v4u sent4 = {SENT, SENT, SENT, SENT};
  const u64 tag0 = *(const __attribute__((address_space(4))) u64*)(a.ws + OFF_EPOCH) + 1ull;
  unsigned tlo = (unsigned)tag0, thi = (unsigned)(tag0 >> 32);
  asm volatile("" : "+s"(tlo), "+s"(thi));
  unsigned one = 1u;
  int saW = lane ? lb + L_JUNK + c16 * 4 : lb + L_SY + (1 - G) * 16 + t4 * 4, saG = lane ? lb + L_JUNK + c16 * 4 : lb + L_SY + 64 + G * 48, r8 = lb + L_SY + (lane & 7) * 4;
  PIN(one); PIN(saW); PIN(saG); PIN(r8);
#define SIG() INC(saW, 0)
#define PUBLISH(addr, OFF, LAST, foff) do { unsigned o_; \
    asm volatile("ds_add_rtn_u32 %0, %1, %2 offset:%3\n\ts_waitcnt lgkmcnt(0)" : "=v"(o_) : "v"(addr), "v"(one), "i"(OFF) : "memory"); \
    if ((unsigned)__builtin_amdgcn_readfirstlane(o_) == (LAST)) __builtin_amdgcn_raw_buffer_store_b64(v2u{tlo, thi}, rs, lane ? OFF_END : (foff), 0, AUX_SC1); \
    CFENCE(); } while (0)
#define SECTION() \
  int bs_ = b_; KA ka = (KA)__builtin_amdgcn_kernarg_segment_ptr(); asm volatile("" : "+s"(bs_), "+s"(ka)); \
  const int xcd2 = bs_ & 7, ci2 = bs_ >> 3, ttB = 8 * xcd2 + (ci2 >> 2), cgB = ci2 & 3, nfB = 8 * cgB + 2 * t4 + G; \
  const bool actB = nfB < 28, actC = ci2 < 28; \
  const int cc = actC ? ci2 : 27, rC = (cc * 37) >> 8, tbD = 4 * (xcd2 >> 1) + rC, jj = cc - 7 * rC, eC = xcd2 & 1, tb8 = 4 * (xcd2 >> 2) + rC, slC = 7 * (xcd2 & 3) + jj, mR = rC * 8 + xcd2; \
  const bool hasR = actC && mR < 28; \
  (void)ttB; (void)nfB; (void)actB; (void)tbD; (void)eC; (void)tb8; (void)slC; (void)hasR; (void)ka

  v4u wc[28], wo[16], wp[2], csf, xr;
  f32x4 c4c = {};
  const int lst = lb + t4 * 7168;
#define WC_DECL() \
  const rsrc_t rwc = RSRC(t4 < 2 ? ka->wg : ka->wu, actC ? DM * DFF * 2 : 0); \
  const int wcv = (lane >> 3) * 3584 + (((((lane & 7) >> 1) ^ (((lane >> 4) & 1) | (t4 & 2))) << 1) | (lane & 1)) * 16; \
  const int wcs = (t4 & 1) * 56 * 3584 + slC * 128
#define WC1(hf, i) DMA(lst, ((hf) ? L_BV : L_BK) + ((i) / 7) * 28672 + ((i) % 7) * 1024, wcv, rwc, wcs + ((2 * (hf) + (i) / 7 + RB0) & 3) * 112 * 3584, ((i) % 7) * 8 * 3584)
#define WO_DECL() \
  const rsrc_t rwo = RSRC(ka->wo, DM * DM * 2); \
  const int wov = (cgB == 3 && (q4 ^ g4) >= 2) ? 0x40000000 : g4 * 896 + (q4 ^ g4) * 64 + p4 * 16; \
  const int wos = t4 * 28 * 896 + cgB * 256
#define WO1(g, i) DMA(lst, ((g) ? L_BK : L_BV) + ((i) / 7) * 28672 + ((i) % 7) * 1024, wov, rwo, wos, ((g) * 224 + ((i) / 7) * 112 + ((i) % 7) * 4) * 896)
#define WC_B(hf, LO, HI) do { _Pragma("unroll") for (int i = LO; i < HI; ++i) WC1(hf, i); } while (0)
#define WO_B(g, LO, HI) do { _Pragma("unroll") for (int i = LO; i < HI; ++i) WO1(g, i); } while (0)
#define XR_LOAD() asm volatile("buffer_load_dwordx4 %0, %1, %2, %3 offen" : "=v"(xr) : "v"(r32 * 896 + h1 * 16), "s"(rx), "s"(ttB * 32 * 896 + (actB ? nfB : 0) * 32) : "memory")
#define PREFILL() do { \
    const int pf1 = l16 + OFF_O + ((tbA * 4 + w) * 16 + hA) * 2048, pf3 = l16 + OFF_H + (ttB * 28 + 8 * cgB + w) * 1024; \
    stc(rs, pf1, 0, sent4); stc(rs, pf1, 1024, sent4); \
    if (!(ttB & 2)) { stc(rs, pf3, 0, sent4); if (cgB < 3) stc(rs, pf3, 4096, sent4); } \
    if (w == 3) stc(rs, l16, lane < 16 ? OFF_HS + (ttB * 4 + cgB) * 256 : OFF_END, sent4); } while (0)
  auto rd_wo = [&](int buf) AI {
    const int ad = buf + (4 * h1 + q4) * 256 + ((t4 ^ q4) * 64) + (blk ^ G) * 32 + p4 * 8;
#pragma unroll
    for (int i = 0; i < 16; ++i) {
      v2u lo = ldtr(L, ad + (28 * (i >> 1) + 16 * (i & 1)) * 256), hi = ldtr(L, ad + (28 * (i >> 1) + 16 * (i & 1) + 8) * 256);
      wo[i] = v4u{lo.x, lo.y, hi.x, hi.y};
    }
  };
  auto fix_wo = [&]() AI {
    if (hh) { wo[15].z = 0u; wo[15].w = 0u; }
    pin_all<0, 16>(wo);
  };
  auto rd_wc = [&](auto HALF) AI {
    constexpr int o = 14 * decltype(HALF)::v;
    const int ad = (o ? L_BV : L_BK) + blk * 14336 + (4 * h1 + q4) * 128 + ((t4 ^ ((q4 >> 1) | (blk << 1))) * 32) + p4 * 8;
#pragma unroll
    for (int i = 0; i < 14; ++i) {
      v2u lo = ldtr(L, ad + (i / 7) * 28672 + (i % 7) * 2048), hi = ldtr(L, ad + (i / 7) * 28672 + (i % 7) * 2048 + 1024);
      wc[o + i] = v4u{lo.x, lo.y, hi.x, hi.y};
    }
  };
  auto cs_wc = [&](auto HALF) AI {
    constexpr int o = 14 * decltype(HALF)::v;
#pragma unroll
    for (int i = 0; i < 14; ++i) SUM4(c4c, wc[o + i]);
    PIN(c4c);
  };
  auto load_wp = [&]() AI {
#pragma unroll
    for (int s = 0; s < 2; ++s) {
      v2u lo = ldtr(L, L_WPHI + (16 * s + 4 * h1 + q4) * 56 + blk * 32 + p4 * 8);
      v2u hi = ldtr(L, L_WPHI + (16 * s + 8 + 4 * h1 + q4) * 56 + blk * 32 + p4 * 8);
      if (s == 1 && hh) hi = v2u{0, 0};
      if (r32 >= 28) { lo = v2u{0, 0}; hi = v2u{0, 0}; }
      wp[s] = v4u{lo.x, lo.y, hi.x, hi.y};
    }
    PIN(wp[0]); PIN(wp[1]);
  };

  if (w < 4) {
    stc(rs, l16, OFF_KVP + (hA * 16 + tbA) * 4096 + w * 1024, sent4);
    if (PFS) { SECTION(); PREFILL(); }
    if (w >= 2) {
      if ((w - 2) * 64 + lane < 98) dma(lb + L_WPHI + (w - 2) * 1024, l16, RSRC(a.wphi, 16 * 28 * 28 * 2), hA * 1568 + (w - 2) * 1024);
    }
    VMW(0);
    ST(2);
    PUBLISH(saG, C_F1, 3, OFF_F1 + b_ * 128);
    ST(3);
    f32x16 acc = {};
    f32x4 c4 = {};
    {
      const int row = 32 * w + r32, key = (row >> 1) & 7;
      int xa[4];
#pragma unroll
      for (int i = 0; i < 4; ++i) xa[i] = L_X + row * 128 + (((2 * i + h1) ^ key) * 16);
      const int wa = L_WK + (8 * h1 + q4) * 64 + blk * 32 + p4 * 8 + ksh;
      bool ahead = false;
      sfor<0, 7>([&](auto S) AI {
        constexpr int s = decltype(S)::v;
        if (!ahead) WAITVK(s + 1, 0);
        v4u xb[4]; v2u wl[4], wh[4];
#pragma unroll
        for (int i = 0; i < 4; ++i) { wl[i] = ldtr(L, wa + s * 4096 + i * 1024); wh[i] = ldtr(L, wa + s * 4096 + i * 1024 + 256); xb[i] = lds128(L, xa[i] + s * 16384); }
        SCHED();
#pragma unroll
        for (int i = 0; i < 4; ++i) { v4u wf = {wl[i].x, wl[i].y, wh[i].x, wh[i].y}; acc = mfma32(wf, xb[i], acc); }
        SIG();
        unsigned pv = *(volatile LAS unsigned*)(L + L_SY + p4 * 4);
        SCHED();
#pragma unroll
        for (int i = 0; i < 4; ++i) { v4u wf = {wl[i].x, wl[i].y, wh[i].x, wh[i].y}; SUM4(c4, wf); }
        PIN(acc); PIN(c4);
        ahead = !__any(pv < s + 2);
        CFENCE();
        if (s == 4) { WAITVK(0, 1); load_wp(); }
      });
    }
    ST(4);
    __builtin_amdgcn_s_setprio(2);
    {
      unsigned hl = hilo(xsum(HS4(c4)));
      PIN(hl);
      v4u cr;
#pragma unroll 1
      for (unsigned sp = 0; sp < LSPIN_CAP; ++sp) {
        unsigned pv = *(volatile LAS unsigned*)(L + L_SY + w * 4);
        cr = *(volatile LAS v4u*)(L + L_COR + w * 512 + r32 * 16);
        if (!__any(pv < 8u)) break;
      }
      ST(29);
      v4u ca = {hh ? 0u : hl, hh ? 0u : hl, 0u, 0u}, cb = {cr.x, cr.y, 0u, 0u};
      acc = mfma32(ca, cb, acc);
      f32x16 ep, en;
      feature_map(acc, __uint_as_float(cr.z), wp, hh, ep, en);
      const int ro = L_PHI + (32 * w + r32) * 128, sw = r32 & 15;
#pragma unroll
      for (int g = 0; g < 4; ++g) {
        *(LAS v2u*)(L + ro + (((2 * g + h1) ^ sw) * 8)) = v2u{pk2(ep[4 * g], ep[4 * g + 1]), pk2(ep[4 * g + 2], ep[4 * g + 3])};
        *(LAS v2u*)(L + ro + (((8 + 2 * g + h1) ^ sw) * 8)) = v2u{pk2(en[4 * g], en[4 * g + 1]), pk2(en[4 * g + 2], en[4 * g + 3])};
      }
      SIG();
    }
    ST(5);
    int f1v; v2u f1;
    f1v = (((c16 >> 2) * 2 + (hA >> 3)) + 8 * ((c16 & 3) * 8 + (hA & 7))) * 128;
    LDA2(f1, f1v, rs, OFF_F1);
    SECTION(); WC_DECL(); WO_DECL();
    const rsrc_t rd = RSRC(ka->wd, hasR ? DM * DFF * 2 : 0);
    {
      const int r0 = 8 * g4 + q4, r1 = r0 + 4;
      int ta0 = L_PHI + r0 * 128 + (((4 * w + p4) ^ (r0 & 15)) * 8), ta1 = L_PHI + r1 * 128 + (((4 * w + p4) ^ (r1 & 15)) * 8);
      int tv = L_V + r0 * 72 + p4 * 8, zo = ((g4 & 1) * 32 + h1 * 16 + c16) * 16 + OFF_KVP + (hA * 16 + tbA) * 4096 + w * 1024;
      PIN(ta0); PIN(ta1); PIN(tv); PIN(zo);
      asm volatile("" ::"s"(rwc), "s"(rwo), "s"(rd), "s"(wcs), "s"(wos), "v"(wcv), "v"(wov));
      WAITVK(11, 8);
      ST(6);
      v2u fa0[4], fa1[4], b00[4], b01[4], b10[4], b11[4];
#pragma unroll
      for (int s = 0; s < 4; ++s) {
        fa0[s] = ldtr(L, ta0 + s * 4096); fa1[s] = ldtr(L, ta1 + s * 4096);
        b00[s] = ldtr(L, tv + s * 4096); b01[s] = ldtr(L, tv + s * 4096 + 288);
        b10[s] = ldtr(L, tv + s * 4096 + 32); b11[s] = ldtr(L, tv + s * 4096 + 288 + 32);
      }
      SCHED();
      f32x4 z0 = {}, z1 = {};
#pragma unroll
      for (int s = 0; s < 4; ++s) {
        const v4u af_ = {fa0[s].x, fa0[s].y, fa1[s].x, fa1[s].y};
        z0 = mfma16(af_, v4u{b00[s].x, b00[s].y, b01[s].x, b01[s].y}, z0);
        z1 = mfma16(af_, v4u{b10[s].x, b10[s].y, b11[s].x, b11[s].y}, z1);
      }
      auto s0 = __builtin_amdgcn_permlane32_swap(pk2(z0.x, z0.y), pk2(z1.x, z1.y), false, false);
      auto s1 = __builtin_amdgcn_permlane32_swap(pk2(z0.z, z0.w), pk2(z1.z, z1.w), false, false);
      stc(rs, zo, 0, guard4(v4u{s0[0], s1[0], s0[1], s1[1]}));
      SCHED();
    }
    ST(7);
    if (!PFS) PREFILL();
    XR_LOAD();
    if (KO1) WO_B(0, 0, 7);
    {
      const int vo = (lane >> 3) * 896 + (lane & 7) * 16, so = (64 * mR + 16 * w) * 896 + 128 * jj, lo_ = lb + w * 2048;
      DMA(lo_, L_RS, vo, rd, so, 0); DMA(lo_, L_RS + 1024, vo, rd, so, 8 * 896);
    }
    {
      v4u kp[16];
      int kso[4];
#pragma unroll
      for (int g = 0; g < 4; ++g) kso[g] = OFF_KVP + hA * 16 * 4096 + w * 1024 + ((g + tbA) & 3) * 16384;
      int kv4[4] = {l16, l16 + 4096, l16 + 8192, l16 + 12288};
      PIN(kv4[1]); PIN(kv4[2]); PIN(kv4[3]);
      v4u sel;
      {
        const unsigned val = (r32 < 16 && ((r32 >> 2) & 1) == h1) ? ((r32 & 1) ? 0x3f800000u : 0x00003f80u) : 0u;
        const int d = ((r32 >> 3) & 1) * 2 + ((r32 >> 1) & 1);
        sel = v4u{d == 0 ? val : 0u, d == 1 ? val : 0u, d == 2 ? val : 0u, d == 3 ? val : 0u};
        PIN(sel);
      }
      VMW(3 + 7 * KO1); PIN(f1);
      if (!PFS) PUBLISH(saG, C_F2, 3, OFF_F2 + bs_ * 128);
      flag_ensure(f1, rs, f1v, OFF_F1, tlo, thi);
      ST(32);
      delay(KDLY);
#define KP_GRP(g) do { LDA4(kp[4 * (g)], kv4[0], rs, kso[g], 0); LDA4(kp[4 * (g) + 1], kv4[1], rs, kso[g], 0); LDA4(kp[4 * (g) + 2], kv4[2], rs, kso[g], 0); LDA4(kp[4 * (g) + 3], kv4[3], rs, kso[g], 0); } while (0)
      KP_GRP(0); KP_GRP(1); KP_GRP(2); KP_GRP(3);
      ST(8);
      WAITVK(13, 0);
      WO_B(0, 7, 14);
      f32x16 ks_ = {};
      sfor<0, 4>([&](auto Gi) AI {
        constexpr int g = decltype(Gi)::v;
        VMW(19 - 4 * g); pin_all<4 * g, 4 * g + 4>(kp);
        if (__builtin_expect(missing<4 * g, 4 * g + 4>(kp), 0)) {
#pragma unroll 1
          for (unsigned sp = 0; sp < SPIN_CAP; ++sp) {
            __builtin_amdgcn_s_sleep(RSLP);
            KP_GRP(g); VMW(0); pin_all<4 * g, 4 * g + 4>(kp);
            if (!missing<4 * g, 4 * g + 4>(kp)) break;
          }
        }
#pragma unroll
        for (int i = 0; i < 4; ++i) ks_ = mfma32(sel, kp[4 * g + i], ks_);
        PIN(ks_);
      });
      *(LAS v4u*)(L + L_KV + w * 1024 + l16) = pack8(ks_, 0);
      SIG();
    }
    __builtin_amdgcn_s_setprio(0);
    PIN(xr);
    ST(9);
    {
      const int a0 = L_RS + w * 2048 + (4 * h1 + q4) * 128 + blk * 32 + p4 * 8;
      const v2u l0 = ldtr(L, a0), h0 = ldtr(L, a0 + 1024), l1 = ldtr(L, a0 + 64), h1_ = ldtr(L, a0 + 1024 + 64);
      const int d0 = hasR ? OFF_WD + (2 * jj * 112 + 4 * mR + w) * 1024 : OFF_END;
      WAITVK(14, 0);
      WC_B(0, 0, 7);
      stc(rs, l16, d0, v4u{l0.x, l0.y, h0.x, h0.y}); stc(rs, l16, d0 + 114688, v4u{l1.x, l1.y, h1_.x, h1_.y});
    }
    VMW(9); SIG();
    WAITVK(0, 10);
    ST(33);
    rd_wo(L_BV);
    SIG();
    WC_B(0, 7, 14);
    fix_wo();
    WAITVK(0, 11);
    WC_B(1, 0, 7);
    VMW(7); SIG();
    PUBLISH(saG, C_FD, 3, OFF_FD + bs_ * 128);
    WC_B(1, 7, 14);
    ST(37);
    WAITVK(0, 12);
    ST(34);
    rd_wc(IC<0>{});
    cs_wc(IC<0>{});
    ST(38);
  } else {
    SEG_ISSUE(1);
    ST(27);
#define WV_ISSUE() do { WAITVK(3, 2); _Pragma("unroll") for (int i = 0; i < 7; ++i) DMA(l1k, L_WV + i * 4096, vk, rqkv, sk, 1792 + 64 * i * 2688); } while (0)
#define WQ1(i) DMA(l1k, L_WQ + (i) * 4096, vk, rqkv, sk, 64 * (i) * 2688)
#define WQ_ISSUE() do { WAITVK(5, 5); _Pragma("unroll") for (int i = 0; i < 7; ++i) WQ1(i); } while (0)
    v4u xf[28], phq[4];
    f32x4 a1 = {}, a2 = {};
    {
      const int row = 32 * t4 + r32, key = (row >> 1) & 7;
      int xa[4];
#pragma unroll
      for (int i = 0; i < 4; ++i) xa[i] = L_X + row * 128 + (((2 * ((i + t4) & 3) + h1) ^ key) * 16);
      sfor<0, 7>([&](auto S) AI {
        constexpr int s = decltype(S)::v;
        VMW((s < 6 ? 5 : 0) + (s > 4 + WVP / 2 ? 7 : 0) + (s > 4 + WQP / 2 ? 7 : 0));
        SIG();
        if (s == 0) ST(3);
        if (s == 3) ST(28);
#pragma unroll
        for (int i = 0; i < 4; ++i) xf[4 * s + i] = lds128(L, xa[i] + s * 16384);
        if constexpr (s < 5) SEG_ISSUE(s + 2);
        if constexpr (2 * (s - 4) == WVP) WV_ISSUE();
        if constexpr (2 * (s - 4) == WQP) WQ_ISSUE();
        SCHED();
#pragma unroll
        for (int i = 0; i < 4; ++i) { SUM4(a1, xf[4 * s + i]); SQ4(a2, xf[4 * s + i]); }
        PIN(a1); PIN(a2);
        SCHED();
        if constexpr (s < 6 && 2 * (s - 4) + 1 == WVP) WV_ISSUE();
        if constexpr (s < 6 && 2 * (s - 4) + 1 == WQP) WQ_ISSUE();
      });
    }
    ST(4);
    __builtin_amdgcn_s_setprio(3);
    const float mean = xsum(HS4(a1)) * (1.f / DM), var = fmaxf(xsum(HS4(a2)) * (1.f / DM) - mean * mean, 0.f);
    const float rstd = __builtin_amdgcn_rsqf(var + 1e-5f);
    const unsigned hl = hilo(-mean);
    const unsigned mh = (hl & 0xffffu) * 0x10001u, ml = (hl >> 16) * 0x10001u;
    *(LAS v4u*)(L + L_COR + t4 * 512 + r32 * 16) = v4u{mh, ml, __float_as_uint(rstd), 0u};
    SIG();
    const v4u mfr = {mh, ml, 0u, 0u};
    ST(5);
    if (WQP == 5) WQ_ISSUE();
    SECTION(); WO_DECL();
    int wa[4];
#pragma unroll
    for (int i = 0; i < 4; ++i) { wa[i] = (8 * h1 + q4) * 64 + blk * 32 + p4 * 8 + ksh + ((i + t4) & 3) * 1024; PIN(wa[i]); }
    const int csa = L_V + 2304 + lane * 4;
    auto gemm_w = [&](auto PH, auto hook) AI -> f32x16 {
      constexpr int ph = decltype(PH)::v, base = ph ? L_WQ : L_WV, P = 5;
      v2u bl[P], bh[P];
      f32x16 acc = {};
      f32x4 c4 = {};
      float cp[4];
      unsigned cx = 0u;
#pragma unroll
      for (int i = 0; i < P; ++i) { bl[i] = ldtr(L, wa[0] + base + i * 4096); bh[i] = ldtr(L, wa[0] + base + i * 4096 + 256); }
      SCHED();
      sfor<0, 28>([&](auto I) AI {
        constexpr int n = decltype(I)::v, m = n + P;
        const v4u wf = {bl[n % P].x, bl[n % P].y, bh[n % P].x, bh[n % P].y};
        acc = mfma32(wf, xf[4 * (n % 7) + n / 7], acc);
        if constexpr (n < 7) { SUM4(c4, wf); PIN(c4); }
        if constexpr (m == 7) { if (ph) WAITVK(11, 0); else WAITVK(9, 7); }
        if constexpr (m < 28) { bl[n % P] = ldtr(L, wa[m / 7] + base + (m % 7) * 4096); bh[n % P] = ldtr(L, wa[m / 7] + base + (m % 7) * 4096 + 256); }
        if constexpr (n == 7) { *(LAS float*)(L + csa + t4 * 4096 + ph * 256) = HS4(c4); SIG(); }
        if constexpr (n == 23) {
          WAITVK(ph ? 12 : 10, 0);
#pragma unroll
          for (int i = 0; i < 4; ++i) cp[i] = *(LAS float*)(L + csa + i * 4096 + ph * 256);
        }
        if constexpr (n == 26) { cx = hilo(xsum((cp[0] + cp[1]) + (cp[2] + cp[3]))); cx = hh ? 0u : cx; PIN(cx); }
        hook(I);
        SCHED();
      });
      PIN(acc);
      acc = mfma32(v4u{cx, cx, 0u, 0u}, mfr, acc);
      PIN(acc);
      return acc;
    };
    VMW(WQP == 6 ? 0 : 7); SIG();
    ST(6);
    __builtin_amdgcn_s_setprio(VGP);
    f32x16 acc = gemm_w(IC<0>{}, [&](auto I) AI { constexpr int n = decltype(I)::v; if constexpr (WQP == 6 && n >= 2 && n < 9) WQ1(n - 2); if constexpr (n >= 23 - NKV && n < 23) WO1(1, n - 23 + NKV); });
    acc *= rstd;
    if (hh) { acc[12] = 1.0f; acc[13] = 0.f; acc[14] = 0.f; acc[15] = 0.f; }
    {
      const int ro = L_V + t4 * 4096 + r32 * 72 + h1 * 8;
#pragma unroll
      for (int g = 0; g < 4; ++g) *(LAS v2u*)(L + ro + g * 16) = v2u{pk2(acc[4 * g], acc[4 * g + 1]), pk2(acc[4 * g + 2], acc[4 * g + 3])};
    }
    VMW(NKV); SIG();
    __builtin_amdgcn_s_setprio(QGP);
    ST(7);
    acc = gemm_w(IC<1>{}, [&](auto I) AI { constexpr int n = decltype(I)::v; if constexpr (n >= QV0 && NKV + n - QV0 < 14) WO1(1, NKV + n - QV0); if constexpr (!KO1 && n >= WV1 && n < WV1 + 7) WO1(0, n - WV1); if constexpr (n == 24) load_wp(); });
    VMW(KO1 ? 0 : 7); SIG();
    ST(34);
    {
      f32x16 ep, en;
      feature_map(acc, rstd, wp, hh, ep, en);
      phq[0] = pack8(ep, 0); phq[1] = pack8(ep, 1); phq[2] = pack8(en, 0); phq[3] = pack8(en, 1);
      pin_all<0, 4>(phq);
    }
    ST(8);
    int f2v; v2u f2;
    {
      const int hd1 = 4 * t4 + p4, tb1 = ttB >> 2;
      const int bA = ((tb1 >> 2) * 2 + (hd1 >> 3)) + 8 * ((tb1 & 3) * 8 + (hd1 & 7));
      const int bH = tb8 + 8 * (8 * t4 + (lane & 7));
      f2v = ((lane & 8) ? bH : bA) * 128;
    }
    constexpr int OFF_FB = PFS ? OFF_F1 : OFF_F2;
    WAITVK(13, 0);
    rd_wo(L_BK);
    VMW(0); SIG();
    fix_wo();
    LDA2(f2, f2v, rs, OFF_FB);
    XR_LOAD();
    int oo = l16 + OFF_O + ((tbA * 4 + t4) * 16 + hA) * 2048;
    PIN(oo);
    ST(33);
    __builtin_amdgcn_s_setprio(3);
    f32x16 o = {};
    {
      v4u kf[4];
#pragma unroll 1
      for (unsigned sp = 0; sp < LSPIN_CAP; ++sp) {
        unsigned pk = *(volatile LAS unsigned*)(L + L_SY + 16 + p4 * 4);
#pragma unroll
        for (int s = 0; s < 4; ++s) kf[s] = *(volatile LAS v4u*)(L + L_KV + s * 1024 + l16);
        if (!__any(pk < 9u)) break;
        __builtin_amdgcn_s_sleep(1);
      }
      ST(9);
#pragma unroll
      for (int s = 0; s < 4; ++s) o = mfma32(kf[s], phq[s], o);
    }
    const float rd = __builtin_amdgcn_rcpf(upper(o[12]) + 1e-6f);
    o *= rd;
    v4u o0 = pack8(o, 0), o1 = pack8(o, 1);
    if (hh) { o1.z = 0; o1.w = 0; }
    stc(rs, oo, 0, guard4(o0)); stc(rs, oo, 1024, guard4(o1));
    SCHED();
    __builtin_amdgcn_s_setprio(2);
    ST(30);
    VMW(2); PIN(xr); PIN(f2);
    flag_ensure(f2, rs, f2v, OFF_FB, tlo, thi);
    if (OW) WAITVK(0, 12);
    ST(31);
    delay(ODLY);
    {
      v4u of[8];
      const int og = OFF_O + ttB * 32768 + t4 * 8192, ol = (t4 < 2 ? L_OK : L_OV - 16384) + t4 * 8192 + l16;
      issue_a<0, 8>(of, rs, l16, og);
      ST(11);
      if (!OW) WAITVK(0, 12);
      rd_wc(IC<0>{});
      if (CSV) cs_wc(IC<0>{});
      ST(35);
      VMW(4); pin_all<0, 4>(of);
      ensure<0, 4>(of, rs, l16, og);
#pragma unroll
      for (int i = 0; i < 4; ++i) *(LAS v4u*)(L + ol + i * 1024) = of[i];
      VMW(0); pin_all<4, 8>(of);
      ensure<4, 8>(of, rs, l16, og);
#pragma unroll
      for (int i = 4; i < 8; ++i) *(LAS v4u*)(L + ol + i * 1024) = of[i];
      SIG();
    }
  }
  SECTION();
  int rbG = lb + L_SY + 64 + G * 48, rb2 = lb + L_SY + 64 + (lane & 1) * 48;
  int oi = l16 + (G ? L_OV : L_OK), po = l16 + (G ? L_OK : L_OV) + t4 * 2048, ho = l16 + (actB ? OFF_H + (ttB * 28 + nfB) * 1024 : OFF_END);
  PIN(rbG); PIN(rb2); PIN(oi); PIN(po); PIN(ho);
  int fdv; v2u fd;
  { int m = (14 * w) / 4 + (lane < 4 ? lane : 4); m = m < 27 ? m : 27; fdv = ((m & 7) + 8 * (7 * (m >> 3) + jj)) * 128; }
  LDA2(fd, fdv, rs, OFF_FD);
  __builtin_amdgcn_s_setprio(1);
  {
    f32x16 acc = {};
    {
      auto x0 = __builtin_amdgcn_permlane32_swap(xr.x, xr.z, false, false);
      auto x1 = __builtin_amdgcn_permlane32_swap(xr.y, xr.w, false, false);
      acc[0] = lo_f(x0[0]); acc[1] = hi_f(x0[0]); acc[2] = lo_f(x1[0]); acc[3] = hi_f(x1[0]);
      acc[4] = lo_f(x0[1]); acc[5] = hi_f(x0[1]); acc[6] = lo_f(x1[1]); acc[7] = hi_f(x1[1]);
      PIN(acc);
    }
    WAITVK(15, 0);
    ST(12);
    v4u ob[NOB];
    unsigned pb;
#pragma unroll
    for (int i = 0; i < NOB; ++i) ob[i] = lds128(L, oi + i * 1024);
    SCHED();
#pragma unroll
    for (int i = 0; i < 16; ++i) {
      acc = mfma32(wo[i], ob[i % NOB], acc);
      if (i + NOB < 16) ob[i % NOB] = lds128(L, oi + (i + NOB) * 1024);
      if (i + NOB == 8) INC(saG, C_B);
      if (i == 13) pb = *(volatile LAS unsigned*)((LAS char*)0 + rbG + C_B);
      SCHED();
    }
    if (__any(pb < 4u)) WAITC(rbG, C_B, 4);
    *(LAS f32x4*)(L + oi + t4 * 2048) = f32x4{acc[8], acc[9], acc[10], acc[11]};
    *(LAS f32x4*)(L + oi + t4 * 2048 + 1024) = f32x4{acc[12], acc[13], acc[14], acc[15]};
    INC(saW, 32);
    f32x4 e0, e1;
#pragma unroll 1
    for (unsigned sp = 0; sp < LSPIN_CAP; ++sp) {
      unsigned pv = *(volatile LAS unsigned*)(L + L_SY + 32 + w * 4);
      e0 = *(volatile LAS f32x4*)(L + po); e1 = *(volatile LAS f32x4*)(L + po + 1024);
      if (!__any(pv < 1u)) break;
    }
    const v4u hv = guard4(v4u{pk2(acc[0] + e0.x, acc[1] + e0.y), pk2(acc[2] + e0.z, acc[3] + e0.w), pk2(acc[4] + e1.x, acc[5] + e1.y), pk2(acc[6] + e1.z, acc[7] + e1.w)});
    stc(rs, ho, 0, hv);
    SCHED();
    ST(13);
    VMW(1); PIN(fd);
    if (!G) { SIG(); }
    f32x4 a1_ = {}, a2_ = {};
    SUM4(a1_, hv); SQ4(a2_, hv);
    *(LAS f32x2*)(L + L_ST2 + r32 * 144 + h1 * 64 + (2 * t4 + G) * 8) = f32x2{HS4(a1_), HS4(a2_)};
  }
  auto hs_pub = [&]() AI {
    WAITC(rb2, C_S, 4);
    const int so = L_ST2 + r32 * 144 + h1 * 64;
    f32x4 p0 = *(LAS f32x4*)(L + so), p1 = *(LAS f32x4*)(L + so + 16), p2 = *(LAS f32x4*)(L + so + 32), p3 = *(LAS f32x4*)(L + so + 48);
    p0 += p1;
    if (cgB < 3) p0 += p2 + p3;
    float s1 = xsum(p0.x + p0.z), s2 = xsum(p0.y + p0.w);
    __builtin_amdgcn_raw_buffer_store_b64(v2u{guard(__float_as_uint(s1)), __float_as_uint(s2)}, rs, hh ? OFF_END : OFF_HS + (ttB * 4 + cgB) * 256 + r32 * 8, 0, AUX_SC1);
  };
  if (bs_ == 0 && tid == 0) *(GAS v2u*)(ka->ws + OFF_EPOCH) = v2u{tlo, thi};
  if (!actC) {
    VMW(0); INC(saG, C_S);
    if (w == 7) hs_pub();
  } else {
    v4u hf[7], hs[7];
    const int tt0 = tb8 * 8 + 4 * G, hsv = r32 * 8 + h1 * 512, hss = OFF_HS + (tt0 + t4) * 1024;
    const int hso = OFF_H + tt0 * 28672 + ((t4 + RB0) & 3) * 7168;
    const int gb = G ? L_BV : L_BK, lcor = (G ? L_COR2 : L_COR) + r32 * 16;
    int lg16 = l16 + gb;
    PIN(lg16);
    const int hlo = t4 * 7168 + lg16, hld = lst + gb;
    auto h_stage = [&](v4u (&hx)[7], auto T) AI {
      constexpr int t = decltype(T)::v;
      pin_all<0, 7>(hx);
      ensure<0, 7>(hx, rs, l16, hso + t * 28672);
#pragma unroll
      for (int i = 0; i < 7; ++i) *(LAS v4u*)(L + hlo + t * 28672 + i * 1024) = hx[i];
      INC(saG, C_H + t * 4);
    };
#define HA(t, ks) (lg16 + ((t) & 1) * 28672 + (ks) * 1024)
    v4u wd[28];
    int rot[7];
#pragma unroll
    for (int p = 0; p < 7; ++p) { int r_ = p + jj; rot[p] = (r_ >= 7 ? r_ - 7 : r_) * 2048 + 14 * w * 1024; }
    const int wdo = OFF_WD + 2 * jj * 112 * 1024;
#define WD_LD(n) wd[n] = ldc(rs, l16 + ((n) & 1) * 1024, wdo + rot[((n) % 14) >> 1] + ((n) >= 14 ? 114688 : 0))
#define WD_LDA(n) LDA4(wd[n], l16, rs, wdo + rot[((n) % 14) >> 1], ((n) & 1) * 1024)
    WAITVK(0, 13);
    rd_wc(IC<1>{});
    INC(saG, C_R);
    flag_ensure(fd, rs, fdv, OFF_FD, tlo, thi);
#pragma unroll
    for (int n = 0; n < NW3; ++n) { WD_LDA(n); if (n == 4) { VMW(5); INC(saG, C_S); } }
    if (NW3 < 5) { VMW(NW3); INC(saG, C_S); }
    if (!CSV && G) cs_wc(IC<0>{});
    if (HCS) { if (w == 7) hs_pub(); cs_wc(IC<1>{}); }
    delay(HDLY);
    issue_a<0, 7>(hf, rs, l16, hso);
    ST(14);
    if (!HCS) { if (w == 7) hs_pub(); cs_wc(IC<1>{}); }
    { unsigned hl = hilo(xsum(HS4(c4c))); hl = hh ? 0u : hl; csf = v4u{hl, hl, 0u, 0u}; PIN(csf); }
    if (!HSL) issue_a<0, 7>(hs, rs, l16, hso + 28672);
    WAITC(rb2, C_R, 4);
    ST(36);
    VMW(HSL ? 0 : 7); h_stage(hf, IC<0>{});
    if (HSL) issue_a<0, 7>(hs, rs, l16, hso + 28672);
    __builtin_amdgcn_s_setprio(0);
    int ao = l16 + OFF_ACT + (tt0 * 112 + 4 * slC + t4) * 1024;
    PIN(ao);
    pin_all<0, NW3>(wd);
    v2u q0 = ldc2(rs, hsv, hss), q1 = ldc2(rs, hsv + 256, hss);
    WAITC(rbG, C_H, 4);
    ST(15);
    f32x16 accA = {}, accB;
    {
      v4u bf[NBF];
#pragma unroll
      for (int i = 0; i < NBF; ++i) bf[i] = lds128(L, HA(0, i));
      SCHED();
#pragma unroll
      for (int ks = 0; ks < 28; ++ks) {
        accA = mfma32(wc[ks], bf[ks % NBF], accA);
        if (ks == (HSL ? 18 : 10)) { PIN(accA); VMW(2); h_stage(hs, IC<1>{}); }
        if (ks == 20) {
          PIN(accA);
#pragma unroll 1
          for (unsigned sp = 0; sp < SPIN_CAP; ++sp) {
            if (__builtin_expect(!__any(max(q0.x, q1.x) == SENT), 1)) break;
            __builtin_amdgcn_s_sleep(RSLP);
            CFENCE();
            q0 = ldc2(rs, hsv, hss); q1 = ldc2(rs, hsv + 256, hss);
          }
          float mean2 = xsum(__uint_as_float(q0.x) + __uint_as_float(q1.x)) * (1.f / DM);
          float var2 = fmaxf(xsum(__uint_as_float(q0.y) + __uint_as_float(q1.y)) * (1.f / DM) - mean2 * mean2, 0.f) + 1e-5f;
          unsigned hl2 = hilo(-mean2);
          *(LAS v4u*)(L + lcor + t4 * 512) = v4u{(hl2 & 0xffffu) * 0x10001u, (hl2 >> 16) * 0x10001u, __float_as_uint(__builtin_amdgcn_rsqf(var2)), __float_as_uint(var2)};
          INC(saG, C_H + 4);
        }
        if (ks + NBF < 28) bf[ks % NBF] = lds128(L, HA(0, ks + NBF));
        if (ks == 27 - NBF) INC(saG, C_D);
        if (ks >= 21 && ks % 2 == 1) stc(rs, ao, (ks / 2 - 10) * 114688, sent4);
        SCHED();
      }
      PIN(accA);
    }
    ST(16);
    constexpr int NPRE = 11, NLDS = 5;
    const int wsl = w == 3 ? 5 : (w == 4 || w == 5) ? w - 1 : w;
    const int wdl = wsl < 5 ? 57344 + wsl * 5120 : 143360 + (wsl - 5) * 5120;
    WAITC(rbG, C_H + 4, 8);
    ST(17);
    const int tE = eC ? 1 : 0, tL = eC ? 2 : 3;
    const int rt = tid >> 8, tok = (tid >> 3) & 31, c8 = tid & 7;
    float k1, k2, vr, tz[8], tn[8]; unsigned ow[4];
    v4u cr = lds128(L, lcor), crn = lds128(L, lcor + 512);
    v4u buf[4];
#pragma unroll
    for (int i = 0; i < 4; ++i) buf[i] = lds128(L, HA(1, i));
    accA = mfma32(csf, v4u{cr.x, cr.y, 0u, 0u}, accA);
    auto epi_consts = [&]() AI {
      const float rr = __uint_as_float(cr.z);
      vr = __uint_as_float(cr.w);
      k1 = (-2.f * 0.7978845608028654f * L2E) * rr; k2 = k1 * 0.044715f * rr * rr;
    };
    auto epi_step = [&](const f32x16& pv, int s) AI {
      const int e = s / 3, ph = s % 3;
      if (ph == 0) { float g = pv[e]; tz[e] = __builtin_fmaf(k2, g * g, k1) * g; PIN(tz[e]); }
      else if (ph == 1) { tz[e] = __builtin_amdgcn_exp2f(tz[e]); tn[e] = pv[e] * pv[e + 8]; tz[e] = __builtin_fmaf(tz[e], vr, vr); PIN(tz[e]); PIN(tn[e]); }
      else { tn[e] *= __builtin_amdgcn_rcpf(tz[e]); if (e & 1) { ow[e >> 1] = pk2(tn[e - 1], tn[e]); if (!(e & 2)) ow[e >> 1] = guard(ow[e >> 1]); PIN(ow[e >> 1]); } else PIN(tn[e]); }
    };
    int f3v; { int j = (14 * w) / 4 + (lane < 4 ? lane : 4); j = j < 27 ? j : 27; f3v = ((tbD >> 1) * 32 + j) * 128; }
    v2u f3, hr0, hr1;
#define NLEFT(u) (NPRE + NLDS - NW3 - (u) * 9 > 6 ? 6 : NPRE + NLDS - NW3 - (u) * 9 < 0 ? 0 : NPRE + NLDS - NW3 - (u) * 9)
#define C_TILE(t, CUR, PRV) do { \
      epi_consts(); \
      CUR = mfma32(csf, v4u{crn.x, crn.y, 0u, 0u}, f32x16{}); \
      cr = crn; \
      if ((t) < 3) crn = lds128(L, lcor + ((t) + 1) * 512); \
      SCHED(); \
      _Pragma("unroll") for (int ks = 0; ks < 28; ++ks) { \
        CUR = mfma32(wc[ks], buf[ks % 4], CUR); \
        if ((t) < 3 && ks == 1) WAITC(rbG, C_D, 4 * (t)); \
        if ((t) < 3 && ks >= 1 && ks < 8) DMAC(hld, (((t) + 1) & 1) * 28672 + (ks - 1) * 1024, l16, rs, hso, ((t) + 1) * 28672 + (ks - 1) * 1024); \
        if ((t) < 3 && ks == 21) { VMW(NLEFT((t) - 1)); INC(saG, C_H + ((t) + 1) * 4); \
          if ((t) == 1) { PIN(CUR); const int sc_ = lane ? saG : lb + SY_C3; PUBLISH(sc_, 0, 7, OFF_F3 + (tb8 * 32 + slC) * 128); } } \
        if ((t) < 3 && ks == 23) WAITC(rbG, C_H + ((t) + 1) * 4, 4); \
        if ((t) < 3 || ks + 4 < 28) buf[ks % 4] = lds128(L, ks + 4 < 28 ? HA(t, ks + 4) : HA((t) + 1, ks - 24)); \
        if (ks >= 2 && ks < 26) epi_step(PRV, ks - 2); \
        if (ks == 26) stc(rs, ao, ((t) - 1) * 114688, v4u{ow[0], ow[1], ow[2], ow[3]}); \
        if ((t) == 1 && ks == 23) INC(saG, C_D); \
        if ((t) < 3 && ks >= 10 && (ks & 1) == 0) { const int q_ = NW3 + ((t) - 1) * 9 + (ks - 10) / 2; \
          if (q_ < NPRE) WD_LD(q_ < NPRE ? q_ : 0); \
          else if (q_ < NPRE + NLDS) { const int n_ = 28 - NLDS + q_ - NPRE; DMAC(lb + wdl, (q_ - NPRE) * 1024, l16, rs, wdo + rot[(n_ % 14) >> 1], (n_ & 1) * 1024 + 114688); } } \
        if ((t) == 3 && ks == 0) f3 = ldc2(rs, f3v, OFF_F3); \
        if ((t) == 3 && (ks & 1) == 1 && NPRE + (ks >> 1) < 28 - NLDS) WD_LD(NPRE + (ks >> 1)); \
        SCHED(); \
      } } while (0)
    if (w >= 4) __builtin_amdgcn_s_setprio(CPRIO);
    C_TILE(1, accB, accA);
    C_TILE(2, accA, accB);
    ST(18);
    C_TILE(3, accB, accA);
    ST(19);
    epi_consts();
#pragma unroll
    for (int s = 0; s < 24; ++s) epi_step(accB, (s % 8) * 3 + s / 8);
    stc(rs, ao, 3 * 114688, v4u{ow[0], ow[1], ow[2], ow[3]});
    SCHED();
    ST(20);
    flag_ensure(f3, rs, f3v, OFF_F3, tlo, thi);
    v4u af[14];
    const int aoE = OFF_ACT + (tbD * 4 + tE) * 114688, aoL = OFF_ACT + (tbD * 4 + tL) * 114688;
#define AF_LD(n, BASE) af[n] = ldc(rs, l16 + ((n) & 1) * 1024, (BASE) + rot[(n) >> 1])
#define AF_ENSURE(LO, HI, BASE) do { \
      if (__builtin_expect(missing<LO, HI>(af), 0)) { \
        _Pragma("unroll 1") for (unsigned sp = 0; sp < SPIN_CAP; ++sp) { \
          __builtin_amdgcn_s_sleep(RSLP); CFENCE(); \
          _Pragma("unroll") for (int n = LO; n < HI; ++n) AF_LD(n, BASE); \
          if (!missing<LO, HI>(af)) break; } } } while (0)
#pragma unroll
    for (int n = 0; n < 14; ++n) AF_LD(n, aoE);
    SCHED();
    VMW(15);
#pragma unroll
    for (int n = 0; n < NLDS; ++n) wd[28 - NLDS + n] = lds128(L, wdl + n * 1024 + l16);
    INC(saG, C_E);
    ST(21);
    auto red_out = [&](int ph, const f32x16& d0, const f32x16& d1) AI {
      const int ro = L_RED + (ph * 8 + w) * 4608 + r32 * 144 + h1 * 8;
#pragma unroll
      for (int g = 0; g < 4; ++g) {
        *(LAS v2u*)(L + ro + g * 16) = v2u{pk2(d0[4 * g], d0[4 * g + 1]), pk2(d0[4 * g + 2], d0[4 * g + 3])};
        *(LAS v2u*)(L + ro + 64 + g * 16) = v2u{pk2(d1[4 * g], d1[4 * g + 1]), pk2(d1[4 * g + 2], d1[4 * g + 3])};
      }
    };
    {
      f32x16 d0 = {}, d1 = {};
      AF_ENSURE(0, 6, aoE);
      {
        const int hv_ = ((rt ? tL : tE) * 28 + (c8 >> 1)) * 1024 + tok * 16 + (c8 & 1) * 8, hb_ = OFF_H + (tbD * 112 + 4 * jj) * 1024;
        hr0 = ldc2(rs, hv_, hb_); hr1 = ldc2(rs, hv_ + 512, hb_);
        SCHED();
      }
#pragma unroll
      for (int n = 0; n < 6; ++n) { d0 = mfma32(wd[n], af[n], d0); d1 = mfma32(wd[14 + n], af[n], d1); SCHED(); }
      PIN(d0); PIN(d1);
      AF_ENSURE(6, 14, aoE);
      WAITC(rb2, C_E, 4);
#pragma unroll
      for (int n = 0; n < 6; ++n) AF_LD(n, aoL);
      SCHED();
#pragma unroll
      for (int n = 6; n < 14; ++n) { d0 = mfma32(wd[n], af[n], d0); d1 = mfma32(wd[14 + n], af[n], d1); AF_LD(n, aoL); SCHED(); }
      red_out(0, d0, d1);
    }
    ST(22);
    KA kb = (KA)__builtin_amdgcn_kernarg_segment_ptr();
    asm volatile("" : "+s"(kb));
    u16* op = kb->out + (size_t)((tbD * 4 + (rt ? tL : tE)) * 32 + tok) * DM + 64 * jj + 8 * c8;
    PIN(op);
    {
      f32x16 d0 = {}, d1 = {};
      AF_ENSURE(0, 6, aoL);
#pragma unroll
      for (int n = 0; n < 6; ++n) { d0 = mfma32(wd[n], af[n], d0); d1 = mfma32(wd[14 + n], af[n], d1); SCHED(); }
      PIN(d0); PIN(d1);
      AF_ENSURE(6, 14, aoL);
      ST(23);
#pragma unroll
      for (int n = 6; n < 14; ++n) { d0 = mfma32(wd[n], af[n], d0); d1 = mfma32(wd[14 + n], af[n], d1); }
      red_out(1, d0, d1);
    }
    ST(24);
    bar();
    {
      float sm[8] = {lo_f(hr0.x), hi_f(hr0.x), lo_f(hr0.y), hi_f(hr0.y), lo_f(hr1.x), hi_f(hr1.x), lo_f(hr1.y), hi_f(hr1.y)};
      v4u v[8];
#pragma unroll
      for (int p = 0; p < 8; ++p) v[p] = lds128(L, L_RED + (rt * 8 + p) * 4608 + tok * 144 + c8 * 16);
#pragma unroll
      for (int p = 0; p < 8; ++p) ACC8(sm, v[p]);
      *(GAS v4u*)op = v4u{pk2(sm[0], sm[1]), pk2(sm[2], sm[3]), pk2(sm[4], sm[5]), pk2(sm[6], sm[7])};
    }
  }
  asm volatile("hk_code_end:");
}
}

extern "C" void kernel_launch(void* const* d_in, const int* in_sizes, int n_in, void* d_out, int out_size, void* d_ws, size_t ws_size, hipStream_t stream) {
  using namespace hk;
  Args a;
  a.x = (const u16*)d_in[0]; a.wqkv = (const u16*)d_in[1]; a.wphi = (const u16*)d_in[2]; a.wo = (const u16*)d_in[3];
  a.wg = (const u16*)d_in[5]; a.wu = (const u16*)d_in[6]; a.wd = (const u16*)d_in[7];
  a.out = (u16*)d_out; a.ws = (char*)d_ws;
  (void)hipFuncSetAttribute((const void*)k_main, hipFuncAttributeMaxDynamicSharedMemorySize, LDS_BYTES);
  k_main<<<256, 512, LDS_BYTES, stream>>>(a);
}
```

```cpp
#include <hip/hip_runtime.h>

namespace hk {
typedef unsigned short u16;
typedef unsigned long long u64;
typedef unsigned v4u __attribute__((ext_vector_type(4)));
typedef unsigned v2u __attribute__((ext_vector_type(2)));
typedef float f32x16 __attribute__((ext_vector_type(16)));
typedef float f32x4 __attribute__((ext_vector_type(4)));
typedef float f32x2 __attribute__((ext_vector_type(2)));
typedef __bf16 bf16x8 __attribute__((ext_vector_type(8)));
typedef __bf16 bf16x2 __attribute__((ext_vector_type(2)));
typedef short s16x4 __attribute__((ext_vector_type(4)));
typedef __amdgpu_buffer_rsrc_t rsrc_t;
#define GAS __attribute__((address_space(1)))
#define LAS __attribute__((address_space(3)))
#define DI __device__ __forceinline__
#define AI __attribute__((always_inline))
#define __uint_as_float(u) __builtin_bit_cast(float, (unsigned)(u))
#define __float_as_uint(f) __builtin_bit_cast(unsigned, (float)(f))
#define __any(p) (__builtin_amdgcn_ballot_w64(p) != 0ull)
#define SCHED() __builtin_amdgcn_sched_barrier(0)
#define CFENCE() asm volatile("" ::: "memory")
#define VMW(n) asm volatile("s_waitcnt vmcnt(%0)" ::"i"(n) : "memory")
#define PIN(x) asm volatile("" : "+v"(x))
#define RSRC(p, n) __builtin_amdgcn_make_buffer_rsrc((void*)(p), 0, (n), 0x00020000)

#define RSLP 2
#define FSLP 4
constexpr int SEQ = 2048, DM = 448, DFF = 1792;
constexpr int OFF_EPOCH = 0, OFF_F1 = 32768, OFF_F2 = 65536, OFF_FD = 98304, OFF_F3 = 131072;
constexpr int OFF_WD = 163840;
constexpr int OFF_KVP = OFF_WD + 14 * 112 * 1024;
constexpr int OFF_O = OFF_KVP + 16 * 16 * 4096;
constexpr int OFF_H = OFF_O + 64 * 16 * 2048;
constexpr int OFF_ACT = OFF_H + 64 * 28 * 1024;
constexpr int OFF_HS = OFF_ACT + 64 * 112 * 1024;
constexpr int OFF_END = OFF_HS + 64 * 1024;
constexpr unsigned SENT = 0xFFFFFFFFu;
constexpr unsigned SPIN_CAP = 1u << 15, LSPIN_CAP = 1u << 21;
constexpr int AUX_SC1 = 16;

constexpr int L_X = 0;
constexpr int L_WK = 114688;
constexpr int L_WPHI = 159744;
constexpr int L_JUNK = L_WPHI + 1600;
constexpr int L_SY = 161600;
constexpr int L_COR = 161792;
constexpr int L_WV = 0, L_WQ = 28672;
constexpr int L_PHI = 143360;
constexpr int L_V = 4 * 16384;
constexpr int L_KV = 5 * 16384;
constexpr int L_BK = 86016, L_BV = 0;
constexpr int L_OK = 57344, L_OV = 143360;
constexpr int L_RS = 57344;
constexpr int L_ST2 = 73728;
constexpr int L_CS = 82944;
constexpr int L_COR2 = 83968;
constexpr int L_RED = 0;
constexpr int LDS_BYTES = 163840;
constexpr int C_H = 0  , C_D = 16, C_B = 20, C_S = 24, C_R = 28, C_E = 32, C_F1 = 36, C_F2 = 40, C_FD = 44, SY_C3 = L_SY + 160;

DI unsigned pk2(float a, float b) { f32x2 v = {a, b}; return __builtin_bit_cast(unsigned, __builtin_convertvector(v, bf16x2)); }
DI float lo_f(unsigned u) { return __uint_as_float(u << 16); }
DI float hi_f(unsigned u) { return __uint_as_float(u & 0xffff0000u); }
DI float dot2(unsigned a, unsigned b, float c) { return __builtin_amdgcn_fdot2_f32_bf16(__builtin_bit_cast(bf16x2, a), __builtin_bit_cast(bf16x2, b), c, false); }
DI f32x16 mfma32(v4u a, v4u b, f32x16 c) { return __builtin_amdgcn_mfma_f32_32x32x16_bf16(__builtin_bit_cast(bf16x8, a), __builtin_bit_cast(bf16x8, b), c, 0, 0, 0); }
DI f32x4 mfma16(v4u a, v4u b, f32x4 c) { return __builtin_amdgcn_mfma_f32_16x16x32_bf16(__builtin_bit_cast(bf16x8, a), __builtin_bit_cast(bf16x8, b), c, 0, 0, 0); }
DI void bar() { asm volatile("s_waitcnt lgkmcnt(0)" ::: "memory"); __builtin_amdgcn_s_barrier(); CFENCE(); }
DI float xsum(float v) { unsigned u = __float_as_uint(v); auto r = __builtin_amdgcn_permlane32_swap(u, u, false, false); return __uint_as_float(r[0]) + __uint_as_float(r[1]); }
DI float xmax(float v) { unsigned u = __float_as_uint(v); auto r = __builtin_amdgcn_permlane32_swap(u, u, false, false); return fmaxf(__uint_as_float(r[0]), __uint_as_float(r[1])); }
DI float xmin(float v) { unsigned u = __float_as_uint(v); auto r = __builtin_amdgcn_permlane32_swap(u, u, false, false); return fminf(__uint_as_float(r[0]), __uint_as_float(r[1])); }
DI float upper(float v) { unsigned u = __float_as_uint(v); auto r = __builtin_amdgcn_permlane32_swap(u, u, false, false); return __uint_as_float(r[1]); }
DI unsigned guard(unsigned u) { return u < 0xFFFFFFFEu ? u : 0xFFFFFFFEu; }
DI v4u guard4(v4u v) { return v4u{guard(v.x), v.y, guard(v.z), v.w}; }
DI unsigned mx4(unsigned m, v4u a) { return max(m, max(a.x, a.z)); }
DI v4u ldc(rsrc_t rs, int voff, int soff) { return __builtin_amdgcn_raw_buffer_load_b128(rs, voff, soff, AUX_SC1); }
DI v2u ldc2(rsrc_t rs, int voff, int soff) { return __builtin_amdgcn_raw_buffer_load_b64(rs, voff, soff, AUX_SC1); }
DI void stc(rsrc_t rs, int voff, int soff, v4u v) { __builtin_amdgcn_raw_buffer_store_b128(v, rs, voff + soff, 0, AUX_SC1); }
DI v4u lds128(LAS char* L, int off) { return *(LAS v4u*)(L + off); }
DI v2u ldtr(LAS char* L, int off) { return __builtin_bit_cast(v2u, __builtin_amdgcn_ds_read_tr16_b64_v4i16((LAS s16x4*)(L + off))); }
DI v4u pack8(const f32x16& a, int s) { return v4u{pk2(a[8 * s], a[8 * s + 1]), pk2(a[8 * s + 2], a[8 * s + 3]), pk2(a[8 * s + 4], a[8 * s + 5]), pk2(a[8 * s + 6], a[8 * s + 7])}; }
#define DMA(lbase, LC, voff, r, sbase, GC) do { int t_; const int da_ = (lbase), db_ = (voff), dc_ = (sbase); const rsrc_t dr_ = (r); \
  asm volatile("s_add_u32 m0, %1, %5\n\ts_add_u32 %0, %4, %6\n\tbuffer_load_dwordx4 %2, %3, %0 offen lds" : "=&s"(t_) : "s"(da_), "v"(db_), "s"(dr_), "s"(dc_), "i"(LC), "i"(GC) : "memory", "scc"); } while (0)
#define DMAC(lbase, LC, voff, r, sbase, GC) do { int t_; \
  asm volatile("s_add_u32 m0, %1, %5\n\ts_add_u32 %0, %4, %6\n\tbuffer_load_dwordx4 %2, %3, %0 offen sc1 lds" : "=&s"(t_) : "s"(lbase), "v"(voff), "s"(r), "s"(sbase), "i"(LC), "i"(GC) : "memory", "scc"); } while (0)
DI void dma(int lds, int voff, rsrc_t r, int soff) {
  asm volatile("s_mov_b32 m0, %0\n\ts_nop 0\n\tbuffer_load_dwordx4 %1, %2, %3 offen lds" ::"s"(lds), "v"(voff), "s"(r), "s"(soff) : "memory");
}
DI void touch(int lds, int voff, rsrc_t r, int soff) {
  asm volatile("s_mov_b32 m0, %0\n\ts_nop 0\n\tbuffer_load_dword %1, %2, %3 offen lds" ::"s"(lds), "v"(voff), "s"(r), "s"(soff) : "memory");
}
#define LDA4(d, voff, r, soff, IMM) asm volatile("buffer_load_dwordx4 %0, %1, %2, %3 offen offset:%4 sc1" : "=v"(d) : "v"(voff), "s"(r), "s"(soff), "i"(IMM) : "memory")
#define LDA2(d, voff, r, soff) asm volatile("buffer_load_dwordx2 %0, %1, %2, %3 offen sc1" : "=v"(d) : "v"(voff), "s"(r), "s"(soff) : "memory")
template <int LO, int HI, int N> DI void issue_a(v4u (&a)[N], rsrc_t rs, int voff, int soff) {
#pragma unroll
  for (int i = LO; i < HI; ++i) LDA4(a[i], voff, rs, soff + (i >> 2) * 4096, (i & 3) * 1024);
}
template <int LO, int HI, int N> DI void pin_all(v4u (&a)[N]) {
#pragma unroll
  for (int i = LO; i < HI; ++i) PIN(a[i]);
}
template <int LO, int HI, int N> DI bool missing(v4u (&a)[N]) {
  unsigned m = max(a[LO].x, a[LO].z);
#pragma unroll
  for (int i = LO + 1; i < HI; ++i) m = mx4(m, a[i]);
  return __any(m == SENT);
}
template <int LO, int HI, int N> DI void ensure(v4u (&a)[N], rsrc_t rs, int voff, int soff) {
  if (__builtin_expect(missing<LO, HI>(a), 0)) {
#pragma unroll 1
    for (unsigned sp = 0; sp < SPIN_CAP; ++sp) {
      __builtin_amdgcn_s_sleep(RSLP);
      issue_a<LO, HI>(a, rs, voff, soff);
      VMW(0); pin_all<LO, HI>(a);
      if (!missing<LO, HI>(a)) break;
    }
  }
}
DI void flag_ensure(v2u f, rsrc_t rs, int voff, int soff, unsigned tlo, unsigned thi) {
  if (__builtin_expect(__any(((f.x ^ tlo) | (f.y ^ thi)) != 0), 0)) {
#pragma unroll 1
    for (unsigned sp = 0; sp < SPIN_CAP; ++sp) {
      __builtin_amdgcn_s_sleep(FSLP);
      CFENCE();
      f = ldc2(rs, voff, soff);
      if (!__any(((f.x ^ tlo) | (f.y ^ thi)) != 0)) break;
    }
  }
  CFENCE();
}
#define INC(addr, OFF) do { const int a_ = (addr); const unsigned o1_ = one; asm volatile("ds_add_u32 %0, %1 offset:%2" ::"v"(a_), "v"(o1_), "i"(OFF) : "memory"); } while (0)
#define WAIT_BODY(RD, PRE, TV) RD "\n\t" PRE "s_mov_b32 %1, 0x200000\n" \
  "1:\n\ts_waitcnt lgkmcnt(0)\n\tv_cmp_gt_u32 vcc, " TV ", %0\n\ts_cbranch_vccz 2f\n\ts_sleep 1\n\t" RD "\n\ts_sub_u32 %1, %1, 1\n\ts_cbranch_scc0 1b\n\ts_waitcnt lgkmcnt(0)\n2:"
#define WAITVK(VT, KT) do { unsigned t_, c_, tv_; \
  asm volatile(WAIT_BODY("ds_read_b32 %0, %3", "v_cndmask_b32_e64 %2, %4, %5, %6\n\t", "%2") : "=&v"(t_), "=&s"(c_), "=&v"(tv_) : "v"(r8), "i"(VT), "i"(KT), "s"(0xF0F0F0F0F0F0F0F0ull) : "memory", "vcc", "scc"); } while (0)
#define WAITC(addr, OFF, T) do { unsigned t_, c_; \
  asm volatile(WAIT_BODY("ds_read_b32 %0, %2 offset:%3", "", "%4") : "=&v"(t_), "=&s"(c_) : "v"(addr), "i"(OFF), "i"(T) : "memory", "vcc", "scc"); } while (0)
DI void delay(int n) { for (int i = 0; i < n; ++i) __builtin_amdgcn_s_sleep(1); }

template <int V> struct IC { static constexpr int v = V; };
template <int I, int N, class F> DI void sfor(F&& f) { if constexpr (I < N) { f(IC<I>{}); sfor<I + 1, N>(f); } }

#define ST(k)

struct Args { const u16 *x, *wqkv, *wphi, *wo, *wg, *wu, *wd; u16* out; char* ws; };
typedef const __attribute__((address_space(4))) Args* KA;

constexpr float L2E = 1.4426950408889634f;
#define SUM4(a, v) do { a.x = dot2(v.x, ONES, a.x); a.y = dot2(v.y, ONES, a.y); a.z = dot2(v.z, ONES, a.z); a.w = dot2(v.w, ONES, a.w); } while (0)
#define SQ4(a, v) do { a.x = dot2(v.x, v.x, a.x); a.y = dot2(v.y, v.y, a.y); a.z = dot2(v.z, v.z, a.z); a.w = dot2(v.w, v.w, a.w); } while (0)
#define HS4(a) ((a.x + a.y) + (a.z + a.w))
#define ACC8(sm, v) do { sm[0] = dot2(v.x, SEL_LO, sm[0]); sm[1] = dot2(v.x, SEL_HI, sm[1]); sm[2] = dot2(v.y, SEL_LO, sm[2]); sm[3] = dot2(v.y, SEL_HI, sm[3]); \
    sm[4] = dot2(v.z, SEL_LO, sm[4]); sm[5] = dot2(v.z, SEL_HI, sm[5]); sm[6] = dot2(v.w, SEL_LO, sm[6]); sm[7] = dot2(v.w, SEL_HI, sm[7]); } while (0)
DI unsigned hilo(float v) { unsigned h = pk2(v, 0.f) & 0xffffu; float r = v - lo_f(h); return h | (pk2(r, 0.f) << 16); }

DI void feature_map(const f32x16& acc, float rstd, const v4u (&wp)[2], bool hh, f32x16& ep, f32x16& en) {
  f32x16 t = acc * rstd;
  v4u t0 = pack8(t, 0), t1 = pack8(t, 1);
  if (hh) { t1.z = 0; t1.w = 0; }
  f32x16 p = {};
  p = mfma32(wp[0], t0, p);
  p = mfma32(wp[1], t1, p);
  SCHED();
  float mx = fmaxf(fmaxf(p[12], p[13]), fmaxf(p[14], p[15])), mn = fminf(fminf(p[12], p[13]), fminf(p[14], p[15]));
  mx = hh ? -__builtin_inff() : mx; mn = hh ? __builtin_inff() : mn;
#pragma unroll
  for (int i = 0; i < 12; i += 2) { mx = fmaxf(fmaxf(mx, p[i]), p[i + 1]); mn = fminf(fminf(mn, p[i]), p[i + 1]); }
  mx = xmax(mx); mn = xmin(mn);
  SCHED();
  float bp = -mx * L2E, bn = mn * L2E;
  float bpp = hh ? -__builtin_inff() : bp, bnp = hh ? -__builtin_inff() : bn;
  float sp0 = 0.f, sp1 = 0.f, sn0 = 0.f, sn1 = 0.f;
#pragma unroll
  for (int q = 0; q < 4; ++q) {
#pragma unroll
    for (int i = 4 * q; i < 4 * q + 4; ++i) {
      ep[i] = __builtin_amdgcn_exp2f(__builtin_fmaf(p[i], L2E, i < 12 ? bp : bpp));
      en[i] = __builtin_amdgcn_exp2f(__builtin_fmaf(p[i], -L2E, i < 12 ? bn : bnp));
    }
    if (q > 0) {
      sp0 += ep[4 * q - 4] + ep[4 * q - 3]; sp1 += ep[4 * q - 2] + ep[4 * q - 1]; sn0 += en[4 * q - 4] + en[4 * q - 3]; sn1 += en[4 * q - 2] + en[4 * q - 1];
    }
    SCHED();
  }
  sp0 += ep[12] + ep[13]; sp1 += ep[14] + ep[15]; sn0 += en[12] + en[13]; sn1 += en[14] + en[15];
  float rp = __builtin_amdgcn_rcpf(xsum(sp0 + sp1)), rn = __builtin_amdgcn_rcpf(xsum(sn0 + sn1));
  ep *= rp;
  en *= rn;
}

#define NKV 14
#define PFS 0
#define THR 8
#define VGP 3
#define QGP 2
#define KDLY 2
#define ODLY 2
#define HDLY 0
#define CPRIO 1
#define NOB 6
#define ROT 0
#define RB0 (ROT ? rC : 0)
#if defined(__HIP_DEVICE_COMPILE__)
#define NOPK __attribute__((target("no-packed-fp32-ops")))
#else
#define NOPK
#endif
#define WV1 16
#define WCV 0
#define WCK 9
#define TWC 1
#define HSL 1
#define HCS 1
#define NW3 0
#define NBF 4
#define WVP 1
#define WQP 5
#define KO1 0
#define QV0 0
#define XD 2
#define OAUX 0
#define NC2 14
#define NB2 14
#define OWN 6
#define HE 0
#define VR0 0
#ifndef NV0
#define NV0 7
#endif
#define NK2 5
#define U3L 0
#define NPRE 11
#define OW 1
#define CSV 1
__global__ void __launch_bounds__(512, 2) NOPK k_main(Args a) {
  extern __shared__ __attribute__((aligned(16))) char smem[];
  LAS char* L = (LAS char*)smem;
  u64 px, pq, pws;
  { KA k0 = (KA)__builtin_amdgcn_kernarg_segment_ptr();
    asm volatile("s_load_dwordx2 %0, %3, 0x0\n\ts_load_dwordx2 %1, %3, 0x8\n\ts_load_dwordx2 %2, %3, 0x40" : "=&s"(px), "=&s"(pq), "=&s"(pws) : "s"(k0) : "memory"); }
  const int lb = (int)(unsigned)(size_t)L;
  const int b = blockIdx.x, xcd = b & 7, ci = b >> 3;
  const int tid = threadIdx.x, lane = tid & 63, w = __builtin_amdgcn_readfirstlane(tid >> 6);
  ST(1);
  if (w == 2) {
    u64 pc; unsigned len;
    asm volatile("s_getpc_b64 %0\nhk_code_begin:\n\ts_mov_b32 %1, hk_code_end-hk_code_begin" : "=s"(pc), "=s"(len));
    touch(lb + L_JUNK, ((ci & 7) * 64 + lane) * 128, RSRC(pc, len), 0);
  }
  if (w == 3 && lane < 48) *(LAS unsigned*)(L + L_SY + lane * 4) = 0u;
  const int t4 = w & 3, G = w >> 2;
  const int tbA = 4 * (xcd >> 1) + (ci >> 3), hA = 8 * (xcd & 1) + (ci & 7);
  const int ksh = hA == 15 ? 8 : 0;
  const int xr_ = lane >> 3;
  int ve = xr_ * 896 + (((lane & 7) ^ (xr_ >> 1)) * 16), vod = ve ^ 64;
  int vk = (lane >> 2) * 2688 + (lane & 3) * 16;
  int sx = (tbA * 128 + 32 * t4) * 896, sk = 16 * t4 * 2688 + 56 * hA - ksh;
  int l1k = lb + t4 * 1024, l4k = lb + t4 * 4096;
  asm volatile("" : "+v"(ve), "+v"(vod), "+v"(vk), "+s"(sx), "+s"(sk), "+s"(l1k), "+s"(l4k));
  asm volatile("s_waitcnt lgkmcnt(0)\n\ts_barrier" : "+s"(px), "+s"(pq), "+s"(pws) : : "memory");
  const rsrc_t rqkv = RSRC(pq, DM * 3 * DM * 2), rx = RSRC(px, SEQ * DM * 2);
#define SEG1(s, i) do { if ((i) == 0) DMA(l1k, L_WK + (s) * 4096, vk, rqkv, sk, 896 + 64 * (s) * 2688); \
    else DMA(l4k, L_X + (s) * 16384 + ((i) - 1) * 1024, ((i) & 1) ? ve : vod, rx, sx, ((i) - 1) * 8 * 896 + (s) * 128); } while (0)
#define SEG_ISSUE(s) do { SEG1(s, 0); SEG1(s, 1); SEG1(s, 2); SEG1(s, 3); SEG1(s, 4); } while (0)
  ST(25);
  if (w >= 4) { SEG_ISSUE(0); SEG_ISSUE(1); if (XD > 2) SEG_ISSUE(2); }
  ST(26);
  int lane_ = lane, b_ = b;
  asm volatile("" : "+v"(lane_), "+s"(b_));
#define lane lane_
  const int r32 = lane & 31; const bool hh = lane >= 32; const int h1 = lane >> 5;
  const int q4 = (lane & 15) >> 2, p4 = lane & 3, blk = (lane >> 4) & 1, g4 = lane >> 4, c16 = lane & 15;
  const rsrc_t rs = RSRC(pws, OFF_END);
  const int l16 = lane * 16;
  unsigned ONES, SEL_LO, SEL_HI;
  asm volatile("s_mov_b32 %0, 0x3f803f80\n\ts_mov_b32 %1, 0x3f80\n\ts_mov_b32 %2, 0x3f800000" : "=s"(ONES), "=s"(SEL_LO), "=s"(SEL_HI));
  const v4u sent4 = {SENT, SENT, SENT, SENT};
  unsigned tlo, thi;
#define TAG() do { const u64 tag0 = *(const volatile __attribute__((address_space(4))) u64*)(pws + OFF_EPOCH) + 1ull; tlo = (unsigned)tag0; thi = (unsigned)(tag0 >> 32); } while (0)
#define TAG_PIN() asm volatile("" : "+s"(tlo), "+s"(thi))
  unsigned one = 1u;
  int jk_ = lb + L_JUNK + c16 * 4, sw_ = lb + L_SY + (1 - G) * 16 + t4 * 4, sg_ = lb + L_SY + 64 + G * 48;
  asm volatile("" : "+v"(jk_), "+s"(sw_), "+s"(sg_));
  int saW = lane ? jk_ : sw_, saG = lane ? jk_ : sg_, r8 = lb + L_SY + (lane & 7) * 4;
  PIN(one); PIN(saW); PIN(saG); PIN(r8);
#define SIG() INC(saW, 0)
#define PUBLISH(addr, OFF, LAST, foff) do { unsigned o_; \
    asm volatile("ds_add_rtn_u32 %0, %1, %2 offset:%3\n\ts_waitcnt lgkmcnt(0)" : "=v"(o_) : "v"(addr), "v"(one), "i"(OFF) : "memory"); \
    if ((unsigned)__builtin_amdgcn_readfirstlane(o_) == (LAST)) __builtin_amdgcn_raw_buffer_store_b64(v2u{tlo, thi}, rs, lane ? OFF_END : (foff), 0, AUX_SC1); \
    CFENCE(); } while (0)
#define SECTION() \
  int bs_ = b_; KA ka = (KA)__builtin_amdgcn_kernarg_segment_ptr(); asm volatile("" : "+s"(bs_), "+s"(ka)); \
  const int xcd2 = bs_ & 7, ci2 = bs_ >> 3, ttB = 8 * xcd2 + (ci2 >> 2), cgB = ci2 & 3, nfB = 8 * cgB + 2 * t4 + G; \
  const bool actB = nfB < 28, actC = ci2 < 28; \
  const int cc = actC ? ci2 : 27, rC = (cc * 37) >> 8, tbD = 4 * (xcd2 >> 1) + rC, jj = cc - 7 * rC, eC = xcd2 & 1, tb8 = 4 * (xcd2 >> 2) + rC, slC = 7 * (xcd2 & 3) + jj, mR = rC * 8 + xcd2; \
  const bool hasR = actC && mR < 28; \
  (void)ttB; (void)nfB; (void)actB; (void)tbD; (void)eC; (void)tb8; (void)slC; (void)hasR; (void)ka

  v4u wc[28], wo[16], wp[2], csf, xr, of[8];
  f32x4 c4c = {};
  const int lst = lb + t4 * 7168;
#define WC_DECL() \
  const rsrc_t rwc = RSRC(t4 < 2 ? ka->wg : ka->wu, actC ? DM * DFF * 2 : 0); \
  const int wcv = (lane >> 3) * 3584 + (((((lane & 7) >> 1) ^ (((lane >> 4) & 1) | (t4 & 2))) << 1) | (lane & 1)) * 16; \
  const int wcs = (t4 & 1) * 56 * 3584 + slC * 128
#define WC1(hf, i) DMA(lst, ((hf) ? L_BV : L_BK) + ((i) / 7) * 28672 + ((i) % 7) * 1024, wcv, rwc, wcs + ((2 * (hf) + (i) / 7 + RB0) & 3) * 112 * 3584, ((i) % 7) * 8 * 3584)
#define WO_DECL() \
  const rsrc_t rwo = RSRC(ka->wo, DM * DM * 2); \
  const int wov = (cgB == 3 && (q4 ^ g4) >= 2) ? 0x40000000 : g4 * 896 + (q4 ^ g4) * 64 + p4 * 16; \
  const int wos = t4 * 28 * 896 + cgB * 256
#define WO1(g, i) DMA(lst, ((g) ? L_BK : L_BV) + ((i) / 7) * 28672 + ((i) % 7) * 1024, wov, rwo, wos, ((g) * 224 + ((i) / 7) * 112 + ((i) % 7) * 4) * 896)
#define WC_B(hf, LO, HI) do { _Pragma("unroll") for (int i = LO; i < HI; ++i) WC1(hf, i); } while (0)
#define WO_B(g, LO, HI) do { _Pragma("unroll") for (int i = LO; i < HI; ++i) WO1(g, i); } while (0)
#define XR_LOAD() asm volatile("buffer_load_dwordx4 %0, %1, %2, %3 offen" : "=v"(xr) : "v"(r32 * 896 + h1 * 16), "s"(rx), "s"(ttB * 32 * 896 + (actB ? nfB : 0) * 32) : "memory")
#define PREFILL() do { \
    const int pf1 = l16 + OFF_O + ((tbA * 4 + w) * 16 + hA) * 2048, pf3 = l16 + OFF_H + (ttB * 28 + 8 * cgB + w) * 1024; \
    stc(rs, pf1, 0, sent4); stc(rs, pf1, 1024, sent4); \
    if (!(ttB & 2)) { stc(rs, pf3, 0, sent4); if (cgB < 3) stc(rs, pf3, 4096, sent4); } \
    if (w == 3) stc(rs, l16, lane < 16 ? OFF_HS + (ttB * 4 + cgB) * 256 : OFF_END, sent4); } while (0)
  auto rd_wo = [&](int buf) AI {
    const int ad = buf + (4 * h1 + q4) * 256 + ((t4 ^ q4) * 64) + (blk ^ G) * 32 + p4 * 8;
#pragma unroll
    for (int i = 0; i < 16; ++i) {
      v2u lo = ldtr(L, ad + (28 * (i >> 1) + 16 * (i & 1)) * 256), hi = ldtr(L, ad + (28 * (i >> 1) + 16 * (i & 1) + 8) * 256);
      wo[i] = v4u{lo.x, lo.y, hi.x, hi.y};
    }
  };
  auto fix_wo = [&]() AI {
    if (hh) { wo[15].z = 0u; wo[15].w = 0u; }
    pin_all<0, 16>(wo);
  };
  auto rd_wc = [&](auto HALF) AI {
    constexpr int o = 14 * decltype(HALF)::v;
    const int ad = (o ? L_BV : L_BK) + blk * 14336 + (4 * h1 + q4) * 128 + ((t4 ^ ((q4 >> 1) | (blk << 1))) * 32) + p4 * 8;
#pragma unroll
    for (int i = 0; i < 14; ++i) {
      v2u lo = ldtr(L, ad + (i / 7) * 28672 + (i % 7) * 2048), hi = ldtr(L, ad + (i / 7) * 28672 + (i % 7) * 2048 + 1024);
      wc[o + i] = v4u{lo.x, lo.y, hi.x, hi.y};
    }
  };
  auto cs_wc = [&](auto HALF) AI {
    constexpr int o = 14 * decltype(HALF)::v;
#pragma unroll
    for (int i = 0; i < 14; ++i) SUM4(c4c, wc[o + i]);
    PIN(c4c);
  };
  auto load_wp = [&]() AI {
#pragma unroll
    for (int s = 0; s < 2; ++s) {
      v2u lo = ldtr(L, L_WPHI + (16 * s + 4 * h1 + q4) * 56 + blk * 32 + p4 * 8);
      v2u hi = ldtr(L, L_WPHI + (16 * s + 8 + 4 * h1 + q4) * 56 + blk * 32 + p4 * 8);
      if (s == 1 && hh) hi = v2u{0, 0};
      if (r32 >= 28) { lo = v2u{0, 0}; hi = v2u{0, 0}; }
      wp[s] = v4u{lo.x, lo.y, hi.x, hi.y};
    }
    PIN(wp[0]); PIN(wp[1]);
  };

  if (w < 4) {
    stc(rs, l16, OFF_KVP + (hA * 16 + tbA) * 4096 + w * 1024, sent4);
    if (PFS) { SECTION(); PREFILL(); }
    if (w >= 2) {
      if ((w - 2) * 64 + lane < 98) dma(lb + L_WPHI + (w - 2) * 1024, l16, RSRC(((KA)__builtin_amdgcn_kernarg_segment_ptr())->wphi, 16 * 28 * 28 * 2), hA * 1568 + (w - 2) * 1024);
    }
    TAG(); TAG_PIN();
    VMW(0);
    ST(2);
    PUBLISH(saG, C_F1, 3, OFF_F1 + b_ * 128);
    ST(3);
    f32x16 acc = {};
    f32x4 c4 = {};
    {
      const int row = 32 * w + r32, key = (row >> 1) & 7;
      int xa[4];
#pragma unroll
      for (int i = 0; i < 4; ++i) xa[i] = L_X + row * 128 + (((2 * i + h1) ^ key) * 16);
      const int wa = L_WK + (8 * h1 + q4) * 64 + blk * 32 + p4 * 8 + ksh;
      bool ahead = false;
      sfor<0, 7>([&](auto S) AI {
        constexpr int s = decltype(S)::v;
        if (!ahead) WAITVK(s + 1, 0);
        v4u xb[4]; v2u wl[4], wh[4];
#pragma unroll
        for (int i = 0; i < 4; ++i) { wl[i] = ldtr(L, wa + s * 4096 + i * 1024); wh[i] = ldtr(L, wa + s * 4096 + i * 1024 + 256); xb[i] = lds128(L, xa[i] + s * 16384); }
        SCHED();
#pragma unroll
        for (int i = 0; i < 4; ++i) { v4u wf = {wl[i].x, wl[i].y, wh[i].x, wh[i].y}; acc = mfma32(wf, xb[i], acc); }
        SIG();
        unsigned pv = *(volatile LAS unsigned*)(L + L_SY + p4 * 4);
        SCHED();
#pragma unroll
        for (int i = 0; i < 4; ++i) { v4u wf = {wl[i].x, wl[i].y, wh[i].x, wh[i].y}; SUM4(c4, wf); }
        PIN(acc); PIN(c4);
        ahead = !__any(pv < s + 2);
        CFENCE();
        if (s == 4) { WAITVK(0, 1); load_wp(); }
      });
    }
    ST(4);
    __builtin_amdgcn_s_setprio(2);
    {
      unsigned hl = hilo(xsum(HS4(c4)));
      PIN(hl);
      v4u cr;
#pragma unroll 1
      for (unsigned sp = 0; sp < LSPIN_CAP; ++sp) {
        unsigned pv = *(volatile LAS unsigned*)(L + L_SY + w * 4);
        cr = *(volatile LAS v4u*)(L + L_COR + w * 512 + r32 * 16);
        if (!__any(pv < 8u)) break;
      }
      ST(29);
      v4u ca = {hh ? 0u : hl, hh ? 0u : hl, 0u, 0u}, cb = {cr.x, cr.y, 0u, 0u};
      acc = mfma32(ca, cb, acc);
      f32x16 ep, en;
      feature_map(acc, __uint_as_float(cr.z), wp, hh, ep, en);
      const int ro = L_PHI + (32 * w + r32) * 128, sw = r32 & 15;
#pragma unroll
      for (int g = 0; g < 4; ++g) {
        *(LAS v2u*)(L + ro + (((2 * g + h1) ^ sw) * 8)) = v2u{pk2(ep[4 * g], ep[4 * g + 1]), pk2(ep[4 * g + 2], ep[4 * g + 3])};
        *(LAS v2u*)(L + ro + (((8 + 2 * g + h1) ^ sw) * 8)) = v2u{pk2(en[4 * g], en[4 * g + 1]), pk2(en[4 * g + 2], en[4 * g + 3])};
      }
      SIG();
    }
    ST(5);
    int f1v; v2u f1;
    f1v = (((c16 >> 2) * 2 + (hA >> 3)) + 8 * ((c16 & 3) * 8 + (hA & 7))) * 128;
    LDA2(f1, f1v, rs, OFF_F1);
    SECTION(); WC_DECL(); WO_DECL();
    const rsrc_t rd = RSRC(ka->wd, hasR ? DM * DFF * 2 : 0);
    {
      const int r0 = 8 * g4 + q4, r1 = r0 + 4;
      int ta0 = L_PHI + r0 * 128 + (((4 * w + p4) ^ (r0 & 15)) * 8), ta1 = L_PHI + r1 * 128 + (((4 * w + p4) ^ (r1 & 15)) * 8);
      int tv = L_V + r0 * 72 + p4 * 8, zo = ((g4 & 1) * 32 + h1 * 16 + c16) * 16 + OFF_KVP + (hA * 16 + tbA) * 4096 + w * 1024;
      PIN(ta0); PIN(ta1); PIN(tv); PIN(zo);
      asm volatile("" ::"s"(rwc), "s"(rwo), "s"(rd), "s"(wcs), "s"(wos), "v"(wcv), "v"(wov));
      WAITVK(11, 8);
      ST(6);
      v2u fa0[4], fa1[4], b00[4], b01[4], b10[4], b11[4];
#pragma unroll
      for (int s = 0; s < 4; ++s) {
        fa0[s] = ldtr(L, ta0 + s * 4096); fa1[s] = ldtr(L, ta1 + s * 4096);
        b00[s] = ldtr(L, tv + s * 4096); b01[s] = ldtr(L, tv + s * 4096 + 288);
        b10[s] = ldtr(L, tv + s * 4096 + 32); b11[s] = ldtr(L, tv + s * 4096 + 288 + 32);
      }
      SCHED();
      f32x4 z0 = {}, z1 = {};
#pragma unroll
      for (int s = 0; s < 4; ++s) {
        const v4u af_ = {fa0[s].x, fa0[s].y, fa1[s].x, fa1[s].y};
        z0 = mfma16(af_, v4u{b00[s].x, b00[s].y, b01[s].x, b01[s].y}, z0);
        z1 = mfma16(af_, v4u{b10[s].x, b10[s].y, b11[s].x, b11[s].y}, z1);
      }
      auto s0 = __builtin_amdgcn_permlane32_swap(pk2(z0.x, z0.y), pk2(z1.x, z1.y), false, false);
      auto s1 = __builtin_amdgcn_permlane32_swap(pk2(z0.z, z0.w), pk2(z1.z, z1.w), false, false);
      stc(rs, zo, 0, guard4(v4u{s0[0], s1[0], s0[1], s1[1]}));
      SCHED();
    }
    ST(7);
    if (!PFS) PREFILL();
#define U3_XR() do { XR_LOAD(); \
      const int vo = (lane >> 3) * 896 + (lane & 7) * 16, so = (64 * mR + 16 * w) * 896 + 128 * jj, lo_ = lb + w * 2048; \
      DMA(lo_, L_RS, vo, rd, so, 0); DMA(lo_, L_RS + 1024, vo, rd, so, 8 * 896); } while (0)
    if (!U3L) U3_XR();
    {
      v4u kp[16];
      int kso[4];
#pragma unroll
      for (int g = 0; g < 4; ++g) kso[g] = OFF_KVP + hA * 16 * 4096 + w * 1024 + ((g + tbA) & 3) * 16384;
      int kv4[4] = {l16, l16 + 4096, l16 + 8192, l16 + 12288};
      PIN(kv4[1]); PIN(kv4[2]); PIN(kv4[3]);
      v4u sel;
      {
        const unsigned val = (r32 < 16 && ((r32 >> 2) & 1) == h1) ? ((r32 & 1) ? 0x3f800000u : 0x00003f80u) : 0u;
        const int d = ((r32 >> 3) & 1) * 2 + ((r32 >> 1) & 1);
        sel = v4u{d == 0 ? val : 0u, d == 1 ? val : 0u, d == 2 ? val : 0u, d == 3 ? val : 0u};
        PIN(sel);
      }
      VMW(U3L ? 0 : 3); PIN(f1);
      if (!PFS) PUBLISH(saG, C_F2, 3, OFF_F2 + bs_ * 128);
      flag_ensure(f1, rs, f1v, OFF_F1, tlo, thi);
      ST(32);
      delay(KDLY);
#define KP_GRP(g) do { LDA4(kp[4 * (g)], kv4[0], rs, kso[g], 0); LDA4(kp[4 * (g) + 1], kv4[1], rs, kso[g], 0); LDA4(kp[4 * (g) + 2], kv4[2], rs, kso[g], 0); LDA4(kp[4 * (g) + 3], kv4[3], rs, kso[g], 0); } while (0)
      KP_GRP(0); KP_GRP(1); KP_GRP(2); KP_GRP(3);
      ST(8);
      WAITVK(13, 0);
      WO_B(0, 7, 14);
      if (U3L) U3_XR();
      f32x16 ks_ = {};
      sfor<0, 4>([&](auto Gi) AI {
        constexpr int g = decltype(Gi)::v;
        VMW(19 + 3 * U3L - 4 * g); pin_all<4 * g, 4 * g + 4>(kp);
        if (__builtin_expect(missing<4 * g, 4 * g + 4>(kp), 0)) {
#pragma unroll 1
          for (unsigned sp = 0; sp < SPIN_CAP; ++sp) {
            __builtin_amdgcn_s_sleep(RSLP);
            KP_GRP(g); VMW(0); pin_all<4 * g, 4 * g + 4>(kp);
            if (!missing<4 * g, 4 * g + 4>(kp)) break;
          }
        }
#pragma unroll
        for (int i = 0; i < 4; ++i) ks_ = mfma32(sel, kp[4 * g + i], ks_);
        PIN(ks_);
      });
      *(LAS v4u*)(L + L_KV + w * 1024 + l16) = pack8(ks_, 0);
      SIG();
    }
    __builtin_amdgcn_s_setprio(0);
    ST(9);
    if (U3L) VMW(0);
    PIN(xr);
    {
      const int a0 = L_RS + w * 2048 + (4 * h1 + q4) * 128 + blk * 32 + p4 * 8;
      const v2u l0 = ldtr(L, a0), h0 = ldtr(L, a0 + 1024), l1 = ldtr(L, a0 + 64), h1_ = ldtr(L, a0 + 1024 + 64);
      const int d0 = hasR ? OFF_WD + (2 * jj * 112 + 4 * mR + w) * 1024 : OFF_END;
      VMW(0); SIG();
      WAITVK(14, 10);
      ST(33);
      rd_wo(L_BV);
      SIG();
      fix_wo();
      WAITVK(15, 11);
      WC_B(0, NV0, 14);
      WC_B(1, 0, NK2);
      VMW(OWN);
      SIG();
      WAITVK(16, 0);
      ST(34);
      WC_B(1, NK2, 14);
      stc(rs, l16, d0, v4u{l0.x, l0.y, h0.x, h0.y}); stc(rs, l16, d0 + 114688, v4u{l1.x, l1.y, h1_.x, h1_.y});
    }
    VMW(16); SIG();
    ST(37);
    if (VR0) { WAITVK(0, 13); rd_wc(IC<0>{}); }
    ST(38);
#pragma unroll
    for (int i = 0; i < 8; ++i) asm volatile("" : "=v"(of[i]));
  } else {
    ST(27);
#define WV_ISSUE() do { WAITVK(3, 2); _Pragma("unroll") for (int i = 0; i < 7; ++i) DMA(l1k, L_WV + i * 4096, vk, rqkv, sk, 1792 + 64 * i * 2688); } while (0)
#define WQ1(i) DMA(l1k, L_WQ + (i) * 4096, vk, rqkv, sk, 64 * (i) * 2688)
#define WQ_ISSUE() do { WAITVK(5, 5); _Pragma("unroll") for (int i = 0; i < 7; ++i) WQ1(i); } while (0)
    v4u xf[28], phq[4];
    f32x4 a1 = {}, a2 = {};
    {
      const int row = 32 * t4 + r32, key = (row >> 1) & 7;
      int xa[4];
#pragma unroll
      for (int i = 0; i < 4; ++i) xa[i] = L_X + row * 128 + (((2 * ((i + t4) & 3) + h1) ^ key) * 16);
      sfor<0, 7>([&](auto S) AI {
        constexpr int s = decltype(S)::v;
        VMW(5 * (6 - s < XD - 1 ? 6 - s : XD - 1) + (s > 4 + WVP / 2 ? 7 : 0) + (s > 4 + WQP / 2 ? 7 : 0));
        SIG();
        if (s == 0) ST(3);
        if (s == 3) ST(28);
#pragma unroll
        for (int i = 0; i < 4; ++i) xf[4 * s + i] = lds128(L, xa[i] + s * 16384);
        if constexpr (2 * (s - 4) == WVP) WV_ISSUE();
        if constexpr (2 * (s - 4) == WQP) WQ_ISSUE();
        if constexpr (s + XD < 7) SEG1(s + XD, 0);
        SCHED();
        sfor<0, 4>([&](auto I) AI {
          constexpr int i = decltype(I)::v;
          if constexpr (s + XD < 7) SEG1(s + XD, i + 1);
          SUM4(a1, xf[4 * s + i]); SQ4(a2, xf[4 * s + i]);
          PIN(a1); PIN(a2);
          SCHED();
        });
        if constexpr (s < 6 && 2 * (s - 4) + 1 == WVP) WV_ISSUE();
        if constexpr (s < 6 && 2 * (s - 4) + 1 == WQP) WQ_ISSUE();
      });
    }
    ST(4);
    __builtin_amdgcn_s_setprio(3);
    const float mean = xsum(HS4(a1)) * (1.f / DM), var = fmaxf(xsum(HS4(a2)) * (1.f / DM) - mean * mean, 0.f);
    const float rstd = __builtin_amdgcn_rsqf(var + 1e-5f);
    const unsigned hl = hilo(-mean);
    const unsigned mh = (hl & 0xffffu) * 0x10001u, ml = (hl >> 16) * 0x10001u;
    *(LAS v4u*)(L + L_COR + t4 * 512 + r32 * 16) = v4u{mh, ml, __float_as_uint(rstd), 0u};
    SIG();
    const v4u mfr = {mh, ml, 0u, 0u};
    ST(5);
    if (WQP == 5) WQ_ISSUE();
    TAG();
    SECTION(); WO_DECL();
    TAG_PIN();
    int wa[4];
#pragma unroll
    for (int i = 0; i < 4; ++i) { wa[i] = (8 * h1 + q4) * 64 + blk * 32 + p4 * 8 + ksh + ((i + t4) & 3) * 1024; PIN(wa[i]); }
    const int csa = L_V + 2304 + lane * 4;
    auto gemm_w = [&](auto PH, auto hook) AI -> f32x16 {
      constexpr int ph = decltype(PH)::v, base = ph ? L_WQ : L_WV, P = 5;
      v2u bl[P], bh[P];
      f32x16 acc = {};
      f32x4 c4 = {};
      float cp[4];
      unsigned cx = 0u;
#pragma unroll
      for (int i = 0; i < P; ++i) { bl[i] = ldtr(L, wa[0] + base + i * 4096); bh[i] = ldtr(L, wa[0] + base + i * 4096 + 256); }
      SCHED();
      sfor<0, 28>([&](auto I) AI {
        constexpr int n = decltype(I)::v, m = n + P;
        const v4u wf = {bl[n % P].x, bl[n % P].y, bh[n % P].x, bh[n % P].y};
        acc = mfma32(wf, xf[4 * (n % 7) + n / 7], acc);
        if constexpr (n < 7) { SUM4(c4, wf); PIN(c4); }
        if constexpr (m == 7) { if (ph) WAITVK(11, 0); else WAITVK(9, 7); }
        if constexpr (m < 28) { bl[n % P] = ldtr(L, wa[m / 7] + base + (m % 7) * 4096); bh[n % P] = ldtr(L, wa[m / 7] + base + (m % 7) * 4096 + 256); }
        if constexpr (n == 7) { *(LAS float*)(L + csa + t4 * 4096 + ph * 256) = HS4(c4); SIG(); }
        if constexpr (n == 23) {
          WAITVK(ph ? 12 : 10, 0);
#pragma unroll
          for (int i = 0; i < 4; ++i) cp[i] = *(LAS float*)(L + csa + i * 4096 + ph * 256);
        }
        if constexpr (n == 26) { cx = hilo(xsum((cp[0] + cp[1]) + (cp[2] + cp[3]))); cx = hh ? 0u : cx; PIN(cx); }
        hook(I);
        SCHED();
      });
      PIN(acc);
      acc = mfma32(v4u{cx, cx, 0u, 0u}, mfr, acc);
      PIN(acc);
      return acc;
    };
    VMW(WQP == 6 ? 0 : 7); SIG();
    ST(6);
    __builtin_amdgcn_s_setprio(VGP);
    f32x16 acc = gemm_w(IC<0>{}, [&](auto I) AI { constexpr int n = decltype(I)::v; if constexpr (WQP == 6 && n >= 2 && n < 9) WQ1(n - 2); if constexpr (n >= 23 - NKV && n < 23) WO1(1, n - 23 + NKV); });
    acc *= rstd;
    if (hh) { acc[12] = 1.0f; acc[13] = 0.f; acc[14] = 0.f; acc[15] = 0.f; }
    {
      const int ro = L_V + t4 * 4096 + r32 * 72 + h1 * 8;
#pragma unroll
      for (int g = 0; g < 4; ++g) *(LAS v2u*)(L + ro + g * 16) = v2u{pk2(acc[4 * g], acc[4 * g + 1]), pk2(acc[4 * g + 2], acc[4 * g + 3])};
    }
    VMW(NKV); SIG();
    __builtin_amdgcn_s_setprio(QGP);
    ST(7);
    acc = gemm_w(IC<1>{}, [&](auto I) AI { constexpr int n = decltype(I)::v; if constexpr (n >= QV0 && NKV + n - QV0 < 14) WO1(1, NKV + n - QV0); if constexpr (!KO1 && n >= WV1 && n < WV1 + 7) WO1(0, n - WV1); if constexpr (n == 24) load_wp(); });
    VMW(KO1 ? 0 : 7); SIG();
    ST(34);
    {
      f32x16 ep, en;
      feature_map(acc, rstd, wp, hh, ep, en);
      phq[0] = pack8(ep, 0); phq[1] = pack8(ep, 1); phq[2] = pack8(en, 0); phq[3] = pack8(en, 1);
      pin_all<0, 4>(phq);
    }
    ST(8);
    int f2v; v2u f2;
    {
      const int hd1 = 4 * t4 + p4, tb1 = ttB >> 2;
      const int bA = ((tb1 >> 2) * 2 + (hd1 >> 3)) + 8 * ((tb1 & 3) * 8 + (hd1 & 7));
      const int bH = tb8 + 8 * (8 * t4 + (lane & 7));
      f2v = ((lane & 8) ? bH : bA) * 128;
    }
    constexpr int OFF_FB = PFS ? OFF_F1 : OFF_F2;
    WAITVK(13, 0);
    rd_wo(L_BK);
    VMW(0); SIG();
    fix_wo();
    if (NV0) { WC_DECL(); WAITVK(14, 0); WC_B(0, 0, NV0); }
    LDA2(f2, f2v, rs, OFF_FB);
    XR_LOAD();
    int oo = l16 + OFF_O + ((tbA * 4 + t4) * 16 + hA) * 2048;
    PIN(oo);
    ST(33);
    __builtin_amdgcn_s_setprio(3);
    f32x16 o = {};
    {
      v4u kf[4];
#pragma unroll 1
      for (unsigned sp = 0; sp < LSPIN_CAP; ++sp) {
        unsigned pk = *(volatile LAS unsigned*)(L + L_SY + 16 + p4 * 4);
#pragma unroll
        for (int s = 0; s < 4; ++s) kf[s] = *(volatile LAS v4u*)(L + L_KV + s * 1024 + l16);
        if (!__any(pk < 9u)) break;
        __builtin_amdgcn_s_sleep(1);
      }
      ST(9);
#pragma unroll
      for (int s = 0; s < 4; ++s) o = mfma32(kf[s], phq[s], o);
    }
    const float rd = __builtin_amdgcn_rcpf(upper(o[12]) + 1e-6f);
    o *= rd;
    v4u o0 = pack8(o, 0), o1 = pack8(o, 1);
    if (hh) { o1.z = 0; o1.w = 0; }
    stc(rs, oo, 0, guard4(o0)); stc(rs, oo, 1024, guard4(o1));
    SCHED();
    SIG();
    __builtin_amdgcn_s_setprio(2);
    ST(30);
    VMW(2); PIN(xr); PIN(f2);
    flag_ensure(f2, rs, f2v, OFF_FB, tlo, thi);
    if (OW) WAITVK(0, 12);
    ST(31);
    delay(ODLY);
    {
      const int og = OFF_O + ttB * 32768 + t4 * 8192;
      issue_a<0, 8>(of, rs, l16, og);
      SIG();
      ST(11);
      if (!OW) WAITVK(0, 10);
      if (VR0) { WAITVK(0, 13); rd_wc(IC<0>{}); }
      ST(35);
    }
  }
  SECTION();
  int rbG = lb + L_SY + 64 + G * 48, rb2 = lb + L_SY + 64 + (lane & 1) * 48;
  int oi = l16 + (G ? L_OV : L_OK), po = l16 + (G ? L_OK : L_OV) + t4 * 2048, ho = l16 + (actB ? OFF_H + (ttB * 28 + nfB) * 1024 : OFF_END);
  int ol = (t4 < 2 ? L_OK : L_OV - 16384) + t4 * 8192 + l16;
  PIN(rbG); PIN(rb2); PIN(oi); PIN(po); PIN(ho); PIN(ol);
  int fdv; v2u fd;
  { int m = (14 * w) / 4 + (lane < 4 ? lane : 4); m = m < 27 ? m : 27; fdv = ((m & 7) + 8 * (7 * (m >> 3) + jj)) * 128; }
  __builtin_amdgcn_s_setprio(1);
  {
    f32x16 acc = {};
    {
      auto x0 = __builtin_amdgcn_permlane32_swap(xr.x, xr.z, false, false);
      auto x1 = __builtin_amdgcn_permlane32_swap(xr.y, xr.w, false, false);
      acc[0] = lo_f(x0[0]); acc[1] = hi_f(x0[0]); acc[2] = lo_f(x1[0]); acc[3] = hi_f(x1[0]);
      acc[4] = lo_f(x0[1]); acc[5] = hi_f(x0[1]); acc[6] = lo_f(x1[1]); acc[7] = hi_f(x1[1]);
      PIN(acc);
    }
    if (G) {
      const int og = OFF_O + ttB * 32768 + t4 * 8192;
      VMW(4); pin_all<0, 4>(of);
      ensure<0, 4>(of, rs, l16, og);
#pragma unroll
      for (int i = 0; i < 4; ++i) *(LAS v4u*)(L + ol + i * 1024) = of[i];
      VMW(0); pin_all<4, 8>(of);
      ensure<4, 8>(of, rs, l16, og);
#pragma unroll
      for (int i = 4; i < 8; ++i) *(LAS v4u*)(L + ol + i * 1024) = of[i];
      SIG();
    }
    WAITVK(17, 0);
    ST(12);
    v4u ob[NOB];
    unsigned pb;
#pragma unroll
    for (int i = 0; i < NOB; ++i) ob[i] = lds128(L, oi + i * 1024);
    SCHED();
#pragma unroll
    for (int i = 0; i < 16; ++i) {
      acc = mfma32(wo[i], ob[i % NOB], acc);
      if (i + NOB < 16) ob[i % NOB] = lds128(L, oi + (i + NOB) * 1024);
      if (i + NOB == 8) INC(saG, C_B);
      if (i == 13) pb = *(volatile LAS unsigned*)((LAS char*)0 + rbG + C_B);
      SCHED();
    }
    if (__any(pb < 4u)) WAITC(rbG, C_B, 4);
    *(LAS f32x4*)(L + oi + t4 * 2048) = f32x4{acc[8], acc[9], acc[10], acc[11]};
    *(LAS f32x4*)(L + oi + t4 * 2048 + 1024) = f32x4{acc[12], acc[13], acc[14], acc[15]};
    INC(saW, 32);
    f32x4 e0, e1;
#pragma unroll 1
    for (unsigned sp = 0; sp < LSPIN_CAP; ++sp) {
      unsigned pv = *(volatile LAS unsigned*)(L + L_SY + 32 + w * 4);
      e0 = *(volatile LAS f32x4*)(L + po); e1 = *(volatile LAS f32x4*)(L + po + 1024);
      if (!__any(pv < 1u)) break;
    }
    const v4u hv = guard4(v4u{pk2(acc[0] + e0.x, acc[1] + e0.y), pk2(acc[2] + e0.z, acc[3] + e0.w), pk2(acc[4] + e1.x, acc[5] + e1.y), pk2(acc[6] + e1.z, acc[7] + e1.w)});
    stc(rs, ho, 0, hv);
    SCHED();
    ST(13);
    VMW(1);
    if (!G) { SIG(); PUBLISH(saG, C_FD, 3, OFF_FD + bs_ * 128); }
    f32x4 a1_ = {}, a2_ = {};
    SUM4(a1_, hv); SQ4(a2_, hv);
    *(LAS f32x2*)(L + L_ST2 + r32 * 144 + h1 * 64 + (2 * t4 + G) * 8) = f32x2{HS4(a1_), HS4(a2_)};
  }
  auto hs_pub = [&]() AI {
    WAITC(rb2, C_S, 4);
    const int so = L_ST2 + r32 * 144 + h1 * 64;
    f32x4 p0 = *(LAS f32x4*)(L + so), p1 = *(LAS f32x4*)(L + so + 16), p2 = *(LAS f32x4*)(L + so + 32), p3 = *(LAS f32x4*)(L + so + 48);
    p0 += p1;
    if (cgB < 3) p0 += p2 + p3;
    float s1 = xsum(p0.x + p0.z), s2 = xsum(p0.y + p0.w);
    __builtin_amdgcn_raw_buffer_store_b64(v2u{guard(__float_as_uint(s1)), __float_as_uint(s2)}, rs, hh ? OFF_END : OFF_HS + (ttB * 4 + cgB) * 256 + r32 * 8, 0, AUX_SC1);
  };
  if (bs_ == 0 && tid == 0) *(GAS v2u*)(ka->ws + OFF_EPOCH) = v2u{tlo, thi};
  if (!actC) {
    VMW(0); INC(saG, C_S);
    if (w == 7) hs_pub();
  } else {
    v4u hf[7], hs[7];
    const int tt0 = tb8 * 8 + 4 * G, hsv = r32 * 8 + h1 * 512, hss = OFF_HS + (tt0 + t4) * 1024;
    const int hso = OFF_H + tt0 * 28672 + ((t4 + RB0) & 3) * 7168;
    const int gb = G ? L_BV : L_BK, lcor = (G ? L_COR2 : L_COR) + r32 * 16;
    int lg16 = l16 + gb;
    PIN(lg16);
    const int hlo = t4 * 7168 + lg16, hld = lst + gb;
    auto h_stage = [&](v4u (&hx)[7], auto T) AI {
      constexpr int t = decltype(T)::v;
      pin_all<0, 7>(hx);
      ensure<0, 7>(hx, rs, l16, hso + t * 28672);
#pragma unroll
      for (int i = 0; i < 7; ++i) *(LAS v4u*)(L + hlo + t * 28672 + i * 1024) = hx[i];
      INC(saG, C_H + t * 4);
    };
#define HA(t, ks) (lg16 + ((t) & 1) * 28672 + (ks) * 1024)
    v4u wd[28];
    int rot[7];
#pragma unroll
    for (int p = 0; p < 7; ++p) { int r_ = p + jj; rot[p] = (r_ >= 7 ? r_ - 7 : r_) * 2048 + 14 * w * 1024; }
    const int wdo = OFF_WD + 2 * jj * 112 * 1024;
#define WD_LD(n) wd[n] = ldc(rs, l16 + ((n) & 1) * 1024, wdo + rot[((n) % 14) >> 1] + ((n) >= 14 ? 114688 : 0))
#define WD_LDA(n) LDA4(wd[n], l16, rs, wdo + rot[((n) % 14) >> 1], ((n) & 1) * 1024)
    if (HE) { delay(HDLY); issue_a<0, 7>(hf, rs, l16, hso); }
    if (!VR0) rd_wc(IC<0>{});
    WAITVK(0, 14);
    rd_wc(IC<1>{});
    INC(saG, C_R);
    VMW(HE ? 7 : 0); INC(saG, C_S);
    if (!HE) { delay(HDLY); issue_a<0, 7>(hf, rs, l16, hso); }
    ST(14);
    if (w == 7) hs_pub();
    if (G) cs_wc(IC<0>{}); else cs_wc(IC<1>{});
    *(LAS float*)(L + L_CS + w * 128 + r32 * 4) = xsum(HS4(c4c));
    INC(saW, 32);
    WAITC(rb2, C_R, 4);
    ST(36);
    VMW(NW3); h_stage(hf, IC<0>{});
    issue_a<0, 7>(hs, rs, l16, hso + 28672);
    __builtin_amdgcn_s_setprio(0);
    int ao = l16 + OFF_ACT + (tt0 * 112 + 4 * slC + t4) * 1024;
    PIN(ao);
    v2u q0 = ldc2(rs, hsv, hss), q1 = ldc2(rs, hsv + 256, hss);
    fd = ldc2(rs, fdv, OFF_FD);
    WAITC(rbG, C_H, 4);
    ST(15);
    f32x16 accA = {}, accB;
    {
      v4u bf[NBF];
      float cs0, cs1;
#pragma unroll
      for (int i = 0; i < NBF; ++i) bf[i] = lds128(L, HA(0, i));
      SCHED();
#pragma unroll
      for (int ks = 0; ks < 28; ++ks) {
        accA = mfma32(wc[ks], bf[ks % NBF], accA);
        if (ks == 18) { PIN(accA); VMW(3); h_stage(hs, IC<1>{}); }
        if (ks == 20) {
          PIN(accA);
#pragma unroll 1
          for (unsigned sp = 0; sp < SPIN_CAP; ++sp) {
            if (__builtin_expect(!__any(max(q0.x, q1.x) == SENT), 1)) break;
            __builtin_amdgcn_s_sleep(RSLP);
            CFENCE();
            q0 = ldc2(rs, hsv, hss); q1 = ldc2(rs, hsv + 256, hss);
          }
          float mean2 = xsum(__uint_as_float(q0.x) + __uint_as_float(q1.x)) * (1.f / DM);
          float var2 = fmaxf(xsum(__uint_as_float(q0.y) + __uint_as_float(q1.y)) * (1.f / DM) - mean2 * mean2, 0.f) + 1e-5f;
          unsigned hl2 = hilo(-mean2);
          *(LAS v4u*)(L + lcor + t4 * 512) = v4u{(hl2 & 0xffffu) * 0x10001u, (hl2 >> 16) * 0x10001u, __float_as_uint(__builtin_amdgcn_rsqf(var2)), __float_as_uint(var2)};
          INC(saG, C_H + 4);
          flag_ensure(fd, rs, fdv, OFF_FD, tlo, thi);
        }
        if (ks == 22) { const int pa_ = lb + L_SY + 32 + w * 4; WAITC(pa_, 0, 2); cs0 = *(LAS float*)(L + L_CS + t4 * 128 + r32 * 4); cs1 = *(LAS float*)(L + L_CS + 512 + t4 * 128 + r32 * 4); }
        if (ks == 25) { unsigned hl = hilo(cs0 + cs1); hl = hh ? 0u : hl; csf = v4u{hl, hl, 0u, 0u}; PIN(csf); }
        if (ks + NBF < 28) bf[ks % NBF] = lds128(L, HA(0, ks + NBF));
        if (ks == 27 - NBF) INC(saG, C_D);
        if (ks >= 21 && ks % 2 == 1) stc(rs, ao, (ks / 2 - 10) * 114688, sent4);
        SCHED();
      }
      PIN(accA);
    }
    ST(16);
    constexpr int NLDS = 5;
    const int wsl = w == 3 ? 5 : (w == 4 || w == 5) ? w - 1 : w;
    const int wdl = wsl < 5 ? 57344 + wsl * 5120 : 143360 + (wsl - 5) * 5120;
    WAITC(rbG, C_H + 4, 8);
    ST(17);
    const int tE = eC ? 1 : 0, tL = eC ? 2 : 3;
    const int rt = tid >> 8, tok = (tid >> 3) & 31, c8 = tid & 7;
    float k1, k2, vr, tz[8], tn[8]; unsigned ow[4];
    v4u cr = lds128(L, lcor), crn = lds128(L, lcor + 512);
    v4u buf[4];
#pragma unroll
    for (int i = 0; i < 4; ++i) buf[i] = lds128(L, HA(1, i));
    accA = mfma32(csf, v4u{cr.x, cr.y, 0u, 0u}, accA);
    auto epi_consts = [&]() AI {
      const float rr = __uint_as_float(cr.z);
      vr = __uint_as_float(cr.w);
      k1 = (-2.f * 0.7978845608028654f * L2E) * rr; k2 = k1 * 0.044715f * rr * rr;
    };
    auto epi_step = [&](const f32x16& pv, int s) AI {
      const int e = s / 3, ph = s % 3;
      if (ph == 0) { float g = pv[e]; tz[e] = __builtin_fmaf(k2, g * g, k1) * g; PIN(tz[e]); }
      else if (ph == 1) { tz[e] = __builtin_amdgcn_exp2f(tz[e]); tn[e] = pv[e] * pv[e + 8]; tz[e] = __builtin_fmaf(tz[e], vr, vr); PIN(tz[e]); PIN(tn[e]); }
      else { tn[e] *= __builtin_amdgcn_rcpf(tz[e]); if (e & 1) { ow[e >> 1] = pk2(tn[e - 1], tn[e]); if (!(e & 2)) ow[e >> 1] = guard(ow[e >> 1]); PIN(ow[e >> 1]); } else PIN(tn[e]); }
    };
    int f3v; { int j = (14 * w) / 4 + (lane < 4 ? lane : 4); j = j < 27 ? j : 27; f3v = ((tbD >> 1) * 32 + j) * 128; }
    v2u f3, hr0, hr1;
#define NLEFT(u) (NPRE + NLDS - NW3 - (u) * 9 > 6 ? 6 : NPRE + NLDS - NW3 - (u) * 9 < 0 ? 0 : NPRE + NLDS - NW3 - (u) * 9)
#define C_TILE(t, CUR, PRV) do { \
      epi_consts(); \
      CUR = mfma32(csf, v4u{crn.x, crn.y, 0u, 0u}, f32x16{}); \
      cr = crn; \
      if ((t) < 3) crn = lds128(L, lcor + ((t) + 1) * 512); \
      SCHED(); \
      _Pragma("unroll") for (int ks = 0; ks < 28; ++ks) { \
        CUR = mfma32(wc[ks], buf[ks % 4], CUR); \
        if ((t) < 3 && ks == 1) WAITC(rbG, C_D, 4 * (t)); \
        if ((t) < 3 && ks >= 1 && ks < 8) DMAC(hld, (((t) + 1) & 1) * 28672 + (ks - 1) * 1024, l16, rs, hso, ((t) + 1) * 28672 + (ks - 1) * 1024); \
        if ((t) < 3 && ks == 21) { VMW(NLEFT((t) - 1)); INC(saG, C_H + ((t) + 1) * 4); \
          if ((t) == 1) { PIN(CUR); const int sc_ = lane ? saG : lb + SY_C3; PUBLISH(sc_, 0, 7, OFF_F3 + (tb8 * 32 + slC) * 128); } } \
        if ((t) < 3 && ks == 23) WAITC(rbG, C_H + ((t) + 1) * 4, 4); \
        if ((t) < 3 || ks + 4 < 28) buf[ks % 4] = lds128(L, ks + 4 < 28 ? HA(t, ks + 4) : HA((t) + 1, ks - 24)); \
        if (ks >= 2 && ks < 26) epi_step(PRV, ks - 2); \
        if (ks == 26) stc(rs, ao, ((t) - 1) * 114688, v4u{ow[0], ow[1], ow[2], ow[3]}); \
        if ((t) == 1 && ks == 23) INC(saG, C_D); \
        if ((t) < 3 && ks >= 10 && (ks & 1) == 0) { const int q_ = NW3 + ((t) - 1) * 9 + (ks - 10) / 2; \
          if (q_ < NPRE) WD_LD(q_ < NPRE ? q_ : 0); \
          else if (q_ < NPRE + NLDS) { const int n_ = 28 - NLDS + q_ - NPRE; DMAC(lb + wdl, (q_ - NPRE) * 1024, l16, rs, wdo + rot[(n_ % 14) >> 1], (n_ & 1) * 1024 + 114688); } } \
        if ((t) == 3 && ks == 0) f3 = ldc2(rs, f3v, OFF_F3); \
        if ((t) == 3 && (ks & 1) == 1 && NPRE + (ks >> 1) < 28 - NLDS) WD_LD(NPRE + (ks >> 1)); \
        SCHED(); \
      } } while (0)
    if (w >= 4) __builtin_amdgcn_s_setprio(CPRIO);
    C_TILE(1, accB, accA);
    C_TILE(2, accA, accB);
    ST(18);
    C_TILE(3, accB, accA);
    ST(19);
    epi_consts();
#pragma unroll
    for (int s = 0; s < 24; ++s) epi_step(accB, (s % 8) * 3 + s / 8);
    stc(rs, ao, 3 * 114688, v4u{ow[0], ow[1], ow[2], ow[3]});
    SCHED();
    ST(20);
    flag_ensure(f3, rs, f3v, OFF_F3, tlo, thi);
    v4u af[14];
    const int aoE = OFF_ACT + (tbD * 4 + tE) * 114688, aoL = OFF_ACT + (tbD * 4 + tL) * 114688;
#define AF_LD(n, BASE) af[n] = ldc(rs, l16 + ((n) & 1) * 1024, (BASE) + rot[(n) >> 1])
#define AF_ENSURE(LO, HI, BASE) do { \
      if (__builtin_expect(missing<LO, HI>(af), 0)) { \
        _Pragma("unroll 1") for (unsigned sp = 0; sp < SPIN_CAP; ++sp) { \
          __builtin_amdgcn_s_sleep(RSLP); CFENCE(); \
          _Pragma("unroll") for (int n = LO; n < HI; ++n) AF_LD(n, BASE); \
          if (!missing<LO, HI>(af)) break; } } } while (0)
#pragma unroll
    for (int n = 0; n < 14; ++n) AF_LD(n, aoE);
    SCHED();
    VMW(15);
#pragma unroll
    for (int n = 0; n < NLDS; ++n) wd[28 - NLDS + n] = lds128(L, wdl + n * 1024 + l16);
    INC(saG, C_E);
    ST(21);
    auto red_out = [&](int ph, const f32x16& d0, const f32x16& d1) AI {
      const int ro = L_RED + (ph * 8 + w) * 4608 + r32 * 144 + h1 * 8;
#pragma unroll
      for (int g = 0; g < 4; ++g) {
        *(LAS v2u*)(L + ro + g * 16) = v2u{pk2(d0[4 * g], d0[4 * g + 1]), pk2(d0[4 * g + 2], d0[4 * g + 3])};
        *(LAS v2u*)(L + ro + 64 + g * 16) = v2u{pk2(d1[4 * g], d1[4 * g + 1]), pk2(d1[4 * g + 2], d1[4 * g + 3])};
      }
    };
    {
      f32x16 d0 = {}, d1 = {};
      AF_ENSURE(0, 6, aoE);
      {
        const int hv_ = ((rt ? tL : tE) * 28 + (c8 >> 1)) * 1024 + tok * 16 + (c8 & 1) * 8, hb_ = OFF_H + (tbD * 112 + 4 * jj) * 1024;
        hr0 = ldc2(rs, hv_, hb_); hr1 = ldc2(rs, hv_ + 512, hb_);
        SCHED();
      }
#pragma unroll
      for (int n = 0; n < 6; ++n) { d0 = mfma32(wd[n], af[n], d0); d1 = mfma32(wd[14 + n], af[n], d1); SCHED(); }
      PIN(d0); PIN(d1);
      AF_ENSURE(6, 14, aoE);
      WAITC(rb2, C_E, 4);
#pragma unroll
      for (int n = 0; n < 6; ++n) AF_LD(n, aoL);
      SCHED();
#pragma unroll
      for (int n = 6; n < 14; ++n) { d0 = mfma32(wd[n], af[n], d0); d1 = mfma32(wd[14 + n], af[n], d1); AF_LD(n, aoL); SCHED(); }
      red_out(0, d0, d1);
    }
    ST(22);
    KA kb = (KA)__builtin_amdgcn_kernarg_segment_ptr();
    asm volatile("" : "+s"(kb));
    const rsrc_t ro_ = RSRC(kb->out, SEQ * DM * 2);
    int ov = ((tbD * 4 + (rt ? tL : tE)) * 32 + tok) * 896 + 128 * jj + 16 * c8;
    PIN(ov);
    asm volatile("" ::"s"(ro_));
    {
      f32x16 d0 = {}, d1 = {};
      AF_ENSURE(0, 6, aoL);
#pragma unroll
      for (int n = 0; n < 6; ++n) { d0 = mfma32(wd[n], af[n], d0); d1 = mfma32(wd[14 + n], af[n], d1); SCHED(); }
      PIN(d0); PIN(d1);
      AF_ENSURE(6, 14, aoL);
      ST(23);
#pragma unroll
      for (int n = 6; n < 14; ++n) { d0 = mfma32(wd[n], af[n], d0); d1 = mfma32(wd[14 + n], af[n], d1); }
      red_out(1, d0, d1);
    }
    ST(24);
    bar();
    {
      float sm[8] = {lo_f(hr0.x), hi_f(hr0.x), lo_f(hr0.y), hi_f(hr0.y), lo_f(hr1.x), hi_f(hr1.x), lo_f(hr1.y), hi_f(hr1.y)};
      v4u v[8];
#pragma unroll
      for (int p = 0; p < 8; ++p) v[p] = lds128(L, L_RED + (rt * 8 + p) * 4608 + tok * 144 + c8 * 16);
#pragma unroll
      for (int p = 0; p < 8; ++p) ACC8(sm, v[p]);
      __builtin_amdgcn_raw_buffer_store_b128(v4u{pk2(sm[0], sm[1]), pk2(sm[2], sm[3]), pk2(sm[4], sm[5]), pk2(sm[6], sm[7])}, ro_, ov, 0, OAUX);
    }
  }
  asm volatile("hk_code_end:");
}
}

extern "C" void kernel_launch(void* const* d_in, const int* in_sizes, int n_in, void* d_out, int out_size, void* d_ws, size_t ws_size, hipStream_t stream) {
  using namespace hk;
  Args a;
  a.x = (const u16*)d_in[0]; a.wqkv = (const u16*)d_in[1]; a.wphi = (const u16*)d_in[2]; a.wo = (const u16*)d_in[3];
  a.wg = (const u16*)d_in[5]; a.wu = (const u16*)d_in[6]; a.wd = (const u16*)d_in[7];
  a.out = (u16*)d_out; a.ws = (char*)d_ws;
  (void)hipFuncSetAttribute((const void*)k_main, hipFuncAttributeMaxDynamicSharedMemorySize, LDS_BYTES);
  k_main<<<256, 512, LDS_BYTES, stream>>>(a);
}
```

```cpp
#include <hip/hip_runtime.h>

namespace hk {
typedef unsigned short u16;
typedef unsigned long long u64;
typedef unsigned v4u __attribute__((ext_vector_type(4)));
typedef unsigned v2u __attribute__((ext_vector_type(2)));
typedef float f32x16 __attribute__((ext_vector_type(16)));
typedef float f32x4 __attribute__((ext_vector_type(4)));
typedef float f32x2 __attribute__((ext_vector_type(2)));
typedef __bf16 bf16x8 __attribute__((ext_vector_type(8)));
typedef __bf16 bf16x2 __attribute__((ext_vector_type(2)));
typedef short s16x4 __attribute__((ext_vector_type(4)));
typedef __amdgpu_buffer_rsrc_t rsrc_t;
#define LAS __attribute__((address_space(3)))
#define DI __device__ __forceinline__
#define AI __attribute__((always_inline))
#define __uint_as_float(u) __builtin_bit_cast(float, (unsigned)(u))
#define __float_as_uint(f) __builtin_bit_cast(unsigned, (float)(f))
#define __any(p) (__builtin_amdgcn_ballot_w64(p) != 0ull)
#define SCHED() __builtin_amdgcn_sched_barrier(0)
#define CFENCE() asm volatile("" ::: "memory")
#define VMW(n) asm volatile("s_waitcnt vmcnt(%0)" ::"i"(n) : "memory")
#define PIN(x) asm volatile("" : "+v"(x))
#define RSRC(p, n) __builtin_amdgcn_make_buffer_rsrc((void*)(p), 0, (n), 0x00020000)

constexpr int SEQ = 2048, DM = 448, DFF = 1792;
constexpr int OFF_EPOCH = 0, OFF_F2 = 65536, OFF_FD = 98304, OFF_F3 = 131072;
constexpr int OFF_WD = 163840;
constexpr int OFF_KVP = OFF_WD + 14 * 112 * 1024;
constexpr int OFF_O = OFF_KVP + 16 * 16 * 4096;
constexpr int OFF_H = OFF_O + 64 * 16 * 2048;
constexpr int OFF_ACT = OFF_H + 64 * 28 * 1024;
constexpr int OFF_HS = OFF_ACT + 64 * 112 * 1024;
constexpr int OFF_END = OFF_HS + 64 * 1024;
constexpr unsigned SENT = 0xFFFFFFFFu;
constexpr unsigned SPIN_CAP = 1u << 15;
constexpr int AUX_SC1 = 16, RSLP = 2, FSLP = 4, NOWHERE = 0x40000000;

constexpr int L_X = 65024;
constexpr int L_WK = 130576;
constexpr int L_WPHI = 159744;
constexpr int L_WPP = 58368;
constexpr int L_JUNK = L_WPHI + 1600;
constexpr int L_SY = 161600;
constexpr int L_COR = 161792;
constexpr int L_WQ = 0, L_WV = 28688;
constexpr int L_PHI = 143360;
constexpr int L_V = 4 * 16384;
constexpr int L_KV = 5 * 16384;
constexpr int L_BK = 86016, L_BV = 0;
constexpr int L_OK = 57344, L_OV = 143360;
constexpr int L_XCH = 73728;
constexpr int L_ST2 = L_OK;
constexpr int L_CS = 82944;
constexpr int L_COR2 = 83968;
constexpr int L_RED = 0;
constexpr int LDS_BYTES = 163840;
constexpr int C_H = 0  , C_E = 20, C_F2 = 24, C_1 = 28, C_2 = 32, C_3 = 36, L_ZERO = L_SY + 96;

DI unsigned pk2(float a, float b) { f32x2 v = {a, b}; return __builtin_bit_cast(unsigned, __builtin_convertvector(v, bf16x2)); }
DI float lo_f(unsigned u) { return __uint_as_float(u << 16); }
DI float hi_f(unsigned u) { return __uint_as_float(u & 0xffff0000u); }
DI float dot2(unsigned a, unsigned b, float c) { return __builtin_amdgcn_fdot2_f32_bf16(__builtin_bit_cast(bf16x2, a), __builtin_bit_cast(bf16x2, b), c, false); }
DI f32x16 mfma32(v4u a, v4u b, f32x16 c) { return __builtin_amdgcn_mfma_f32_32x32x16_bf16(__builtin_bit_cast(bf16x8, a), __builtin_bit_cast(bf16x8, b), c, 0, 0, 0); }
DI f32x4 mfma16(v4u a, v4u b, f32x4 c) { return __builtin_amdgcn_mfma_f32_16x16x32_bf16(__builtin_bit_cast(bf16x8, a), __builtin_bit_cast(bf16x8, b), c, 0, 0, 0); }
DI f32x4 mfma16s(v2u a, v2u b, f32x4 c) { return __builtin_amdgcn_mfma_f32_16x16x16bf16_1k(__builtin_bit_cast(s16x4, a), __builtin_bit_cast(s16x4, b), c, 0, 0, 0); }
DI float xsum16(float v) { unsigned u = __float_as_uint(v); auto r = __builtin_amdgcn_permlane16_swap(u, u, false, false); return __uint_as_float(r[0]) + __uint_as_float(r[1]); }
struct Acc { f32x4 a[2][2]; };
DI void bar() { asm volatile("s_waitcnt lgkmcnt(0)" ::: "memory"); __builtin_amdgcn_s_barrier(); CFENCE(); }
DI float xsum(float v) { unsigned u = __float_as_uint(v); auto r = __builtin_amdgcn_permlane32_swap(u, u, false, false); return __uint_as_float(r[0]) + __uint_as_float(r[1]); }
DI float xmax(float v) { unsigned u = __float_as_uint(v); auto r = __builtin_amdgcn_permlane32_swap(u, u, false, false); return fmaxf(__uint_as_float(r[0]), __uint_as_float(r[1])); }
DI float xmin(float v) { unsigned u = __float_as_uint(v); auto r = __builtin_amdgcn_permlane32_swap(u, u, false, false); return fminf(__uint_as_float(r[0]), __uint_as_float(r[1])); }
DI float upper(float v) { unsigned u = __float_as_uint(v); auto r = __builtin_amdgcn_permlane32_swap(u, u, false, false); return __uint_as_float(r[1]); }
DI unsigned guard(unsigned u) { return u < 0xFFFFFFFEu ? u : 0xFFFFFFFEu; }
DI unsigned mx4(unsigned m, v4u a) { return max(m, max(a.x, a.z)); }
DI v2u ldc2(rsrc_t rs, int voff, int soff) { return __builtin_amdgcn_raw_buffer_load_b64(rs, voff, soff, AUX_SC1); }
DI void stc(rsrc_t rs, int voff, int soff, v4u v) { __builtin_amdgcn_raw_buffer_store_b128(v, rs, voff + soff, 0, AUX_SC1); }
DI v4u lds128(LAS char* L, int off) { return *(LAS v4u*)(L + off); }
DI v2u ldtr(LAS char* L, int off) { return __builtin_bit_cast(v2u, __builtin_amdgcn_ds_read_tr16_b64_v4i16((LAS s16x4*)(L + off))); }
DI v4u pack8(const f32x16& a, int s) { return v4u{pk2(a[8 * s], a[8 * s + 1]), pk2(a[8 * s + 2], a[8 * s + 3]), pk2(a[8 * s + 4], a[8 * s + 5]), pk2(a[8 * s + 6], a[8 * s + 7])}; }
#define DMA(lbase, LC, voff, r, sbase, GC) do { int t_; const int da_ = (lbase), db_ = (voff), dc_ = (sbase); const auto dr_ = (r); \
  asm volatile("s_add_u32 m0, %1, %5\n\ts_add_u32 %0, %4, %6\n\tbuffer_load_dwordx4 %2, %3, %0 offen lds" : "=&s"(t_) : "s"(da_), "v"(db_), "s"(dr_), "s"(dc_), "i"(LC), "i"(GC) : "memory", "scc"); } while (0)
#define DMAC(lbase, LC, voff, r, sbase, GC) do { int t_; \
  asm volatile("s_add_u32 m0, %1, %5\n\ts_add_u32 %0, %4, %6\n\tbuffer_load_dwordx4 %2, %3, %0 offen sc1 lds" : "=&s"(t_) : "s"(lbase), "v"(voff), "s"(r), "s"(sbase), "i"(LC), "i"(GC) : "memory", "scc"); } while (0)
DI void dma(int lds, int voff, rsrc_t r, int soff) {
  asm volatile("s_mov_b32 m0, %0\n\ts_nop 0\n\tbuffer_load_dwordx4 %1, %2, %3 offen lds" ::"s"(lds), "v"(voff), "s"(r), "s"(soff) : "memory");
}
DI void touch(int lds, int voff, rsrc_t r, int soff) {
  asm volatile("s_mov_b32 m0, %0\n\ts_nop 0\n\tbuffer_load_dword %1, %2, %3 offen lds" ::"s"(lds), "v"(voff), "s"(r), "s"(soff) : "memory");
}
#define LDA4(d, voff, r, soff, IMM) asm volatile("buffer_load_dwordx4 %0, %1, %2, %3 offen offset:%4 sc1" : "=v"(d) : "v"(voff), "s"(r), "s"(soff), "i"(IMM) : "memory")
#define LDA2(d, voff, r, soff) asm volatile("buffer_load_dwordx2 %0, %1, %2, %3 offen sc1" : "=v"(d) : "v"(voff), "s"(r), "s"(soff) : "memory")
template <int LO, int HI, int N> DI void pin_all(v4u (&a)[N]) {
#pragma unroll
  for (int i = LO; i < HI; ++i) PIN(a[i]);
}
template <int LO, int HI, int N> DI bool missing(v4u (&a)[N]) {
  unsigned m = max(a[LO].x, a[LO].z);
#pragma unroll
  for (int i = LO + 1; i < HI; ++i) m = mx4(m, a[i]);
  return __any(m == SENT);
}
template <int LO, int HI, int N> DI bool untagged(v4u (&a)[N], unsigned tag) {
  unsigned m = a[LO].w ^ tag;
#pragma unroll
  for (int i = LO + 1; i < HI; ++i) m |= a[i].w ^ tag;
  return __any(m > 0xffffu);
}
template <int LO, int HI> DI unsigned lds_max(LAS char* L, int la) {
  v4u t[HI - LO];
#pragma unroll
  for (int i = LO; i < HI; ++i) t[i - LO] = lds128(L, la + i * 1024);
  unsigned m = max(t[0].x, t[0].z);
#pragma unroll
  for (int i = 1; i < HI - LO; ++i) m = mx4(m, t[i]);
  return m;
}
template <int LO, int HI> DI unsigned lds_tag(LAS char* L, int la, unsigned tag) {
  unsigned t[HI - LO], m = 0u;
#pragma unroll
  for (int i = LO; i < HI; ++i) t[i - LO] = *(LAS unsigned*)(L + la + i * 1024 + 12);
#pragma unroll
  for (int i = 0; i < HI - LO; ++i) m |= t[i] ^ tag;
  return m;
}
#define GATHER(N, lbase, LC, so, GC) do { _Pragma("unroll") for (int i_ = 0; i_ < (N); ++i_) DMAC(lbase, (LC) + i_ * 1024, l16, rs, so, (GC) + i_ * 1024); } while (0)
#define REGATHER(N, lbase, la, LC, so, GC) do { _Pragma("unroll 1") for (unsigned sp = 0; sp < SPIN_CAP; ++sp) { \
    __builtin_amdgcn_s_sleep(RSLP); GATHER(N, lbase, LC, so, GC); VMW(0); if (!__any((lds_max<0, N>(L, (la) + (LC))) == SENT)) break; } } while (0)
DI void flag_ensure(v2u f, rsrc_t rs, int voff, int soff, unsigned tlo, unsigned thi) {
  if (__builtin_expect(__any(((f.x ^ tlo) | (f.y ^ thi)) != 0), 0)) {
#pragma unroll 1
    for (unsigned sp = 0; sp < SPIN_CAP; ++sp) {
      __builtin_amdgcn_s_sleep(FSLP);
      CFENCE();
      f = ldc2(rs, voff, soff);
      if (!__any(((f.x ^ tlo) | (f.y ^ thi)) != 0)) break;
    }
  }
  CFENCE();
}
#define INC(addr, OFF) do { const int a_ = (addr); const unsigned o1_ = one; asm volatile("ds_add_u32 %0, %1 offset:%2" ::"v"(a_), "v"(o1_), "i"(OFF) : "memory"); } while (0)
#define WAITC(addr, OFF, T) do { unsigned t_, c_; const int a_ = (addr); \
  asm volatile("ds_read_b32 %0, %2 offset:%3\n\ts_mov_b32 %1, 0x200000\n" \
    "1:\n\ts_waitcnt lgkmcnt(0)\n\tv_cmp_gt_u32 vcc, %4, %0\n\ts_cbranch_vccz 2f\n\ts_sleep 1\n\tds_read_b32 %0, %2 offset:%3\n\ts_sub_u32 %1, %1, 1\n\ts_cbranch_scc0 1b\n\ts_waitcnt lgkmcnt(0)\n2:" \
    : "=&v"(t_), "=&s"(c_) : "v"(a_), "i"(OFF), "i"(T) : "memory", "vcc", "scc"); } while (0)
#define FAIR(i, a, b, c) do { if ((i) == (a)) __builtin_amdgcn_s_setprio(2); if ((i) == (b)) __builtin_amdgcn_s_setprio(1); if ((i) == (c)) __builtin_amdgcn_s_setprio(0); } while (0)
DI void delay(int n) { for (int i = 0; i < n; ++i) __builtin_amdgcn_s_sleep(1); }

template <int V> struct IC { static constexpr int v = V; };
template <int I, int N, class F> DI void sfor(F&& f) { if constexpr (I < N) { f(IC<I>{}); sfor<I + 1, N>(f); } }

#define ST(k)
#define STM(c, k)

struct Args { const u16 *x, *wqkv, *wphi, *wo, *wg, *wu, *wd; u16* out; char* ws; };
typedef const __attribute__((address_space(4))) Args* KA;

constexpr float L2E = 1.4426950408889634f;
#define SUM4(a, v) do { a.x = dot2(v.x, ONES, a.x); a.y = dot2(v.y, ONES, a.y); a.z = dot2(v.z, ONES, a.z); a.w = dot2(v.w, ONES, a.w); } while (0)
#define SQ4(a, v) do { a.x = dot2(v.x, v.x, a.x); a.y = dot2(v.y, v.y, a.y); a.z = dot2(v.z, v.z, a.z); a.w = dot2(v.w, v.w, a.w); } while (0)
DI float add2(float a, float b) { PIN(a); return a + b; }
#define HS4(a) add2(a.x + a.y, a.z + a.w)
DI unsigned hilo(float v) { return pk2(v, v - hi_f(pk2(v, v))); }
DI v2u hhll(float v) { const unsigned h = pk2(v, v); const float r = v - hi_f(h); return v2u{h, pk2(r, r)}; }
DI float add4(const float (&c)[4]) { float a = c[0] + c[1], b = c[2] + c[3]; PIN(a); return a + b; }
DI float xsum_up(float v) { auto r = __builtin_amdgcn_permlane32_swap(0u, __float_as_uint(v), false, false); return __uint_as_float(r[0]) + __uint_as_float(r[1]); }
DI f32x16 mfma32s(v2u a, v2u b, f32x16 c) { return __builtin_amdgcn_mfma_f32_32x32x8bf16_1k(__builtin_bit_cast(s16x4, a), __builtin_bit_cast(s16x4, b), c, 0, 0, 0); }

DI f32x16 feature_proj(const f32x16& acc, const v4u (&wp)[2], bool hh) {
  v4u t0 = pack8(acc, 0), t1 = pack8(acc, 1);
  if (hh) { t1.z = 0; t1.w = 0; }
  f32x16 p = {};
  p = mfma32(wp[0], t0, p);
  p = mfma32(wp[1], t1, p);
  return p;
}
DI f32x2 fma2(f32x2 a, float b, float c) { return __builtin_elementwise_fma(a, f32x2{b, b}, f32x2{c, c}); }
template <class F> DI void feature_map(const f32x16& p, float rstd, unsigned (&op)[8], unsigned (&on)[8], F&& hook) {
  const float cl = rstd * L2E;
  SCHED();
  float mx = fmaxf(p[6], p[14]), mn = fminf(p[6], p[14]);
#pragma unroll
  for (int i = 0; i < 14; i += 2) if (i != 6) { mx = fmaxf(fmaxf(mx, p[i]), p[i + 1]); mn = fminf(fminf(mn, p[i]), p[i + 1]); }
  mx = xmax(mx); mn = xmin(mn);
  SCHED();
  const float bp = -mx * cl, bn = mn * cl;
  f32x2 ep[7], en[7], sp, sn;
#pragma unroll
  for (int i = 0; i < 7; ++i) {
    f32x2 pp = {p[2 * i], p[i == 3 ? 14 : 2 * i + 1]};
    if (i == 3) PIN(pp);
    const f32x2 a = fma2(pp, cl, bp), b = fma2(pp, -cl, bn);
    ep[i] = f32x2{__builtin_amdgcn_exp2f(a.x), __builtin_amdgcn_exp2f(a.y)};
    en[i] = f32x2{__builtin_amdgcn_exp2f(b.x), __builtin_amdgcn_exp2f(b.y)};
    PIN(ep[i]); PIN(en[i]);
    hook(i);
    if (i == 2) { sp = ep[0] + ep[1]; sn = en[0] + en[1]; }
    if (i > 2) { sp += ep[i - 1]; sn += en[i - 1]; }
  }
  sp += ep[6]; sn += en[6];
  const float rp = __builtin_amdgcn_rcpf(xsum(add2(sp.x, sp.y))), rn = __builtin_amdgcn_rcpf(xsum(add2(sn.x, sn.y)));
#pragma unroll
  for (int i = 0; i < 7; ++i) {
    op[i] = __builtin_bit_cast(unsigned, __builtin_convertvector(ep[i] * rp, bf16x2));
    on[i] = __builtin_bit_cast(unsigned, __builtin_convertvector(en[i] * rn, bf16x2));
  }
  op[7] = op[3] >> 16; op[3] &= 0xffffu; on[7] = on[3] >> 16; on[3] &= 0xffffu;
}

constexpr int NQ5 = 5;
constexpr int NTQ = 11;
constexpr int KDLY = 4, ODLY = 2;
constexpr int H1A = 3;
constexpr int NPRE = 13, NLDS = 5;
constexpr int NAF = 6, NE3 = 4;
constexpr int af_cnt(int p) {
  const int c = NAF + 2 * (p + 1);
  return c < 28 ? c : 28;
}

__global__ void __launch_bounds__(512, 2) k_main(Args a) {
  extern __shared__ __attribute__((aligned(16))) char smem[];
  LAS char* L = (LAS char*)0;
  asm volatile("" ::"s"((LAS char*)smem));
  u64 px, pq, pws;
  { KA k0 = (KA)__builtin_amdgcn_kernarg_segment_ptr();
    asm volatile("s_load_dwordx2 %0, %3, 0x0\n\ts_load_dwordx2 %1, %3, 0x8\n\ts_load_dwordx2 %2, %3, 0x40" : "=&s"(px), "=&s"(pq), "=&s"(pws) : "s"(k0) : "memory"); }
  const int b = blockIdx.x, xcd = b & 7, ci = b >> 3;
  const int tid = threadIdx.x, lane = tid & 63, w = __builtin_amdgcn_readfirstlane(tid >> 6);
  ST(1);
  if (w == 2) {
    u64 pc; unsigned len;
    asm volatile("s_getpc_b64 %0\nhk_code_begin:\n\ts_mov_b32 %1, hk_code_end-hk_code_begin" : "=s"(pc), "=s"(len));
    touch(L_JUNK, ((ci & 7) * 64 + lane) * 128, RSRC(pc, len), 0);
  }
  const int t4 = w & 3, G = w >> 2;
#define HPERM(g, q) ((((g) ? 0x75643120u : 0x76534210u) >> (4 * (q))) & 7)
  const int tbA = 4 * (xcd >> 1) + (ci & 3), hA = 8 * (xcd & 1) + (int)HPERM(xcd & 1, ci >> 2);
  const int ksh = hA == 15 ? 8 : 0;
  const int xr_ = lane >> 3;
  int ve = xr_ * 896 + (((lane & 7) ^ (xr_ >> 1)) * 16), vod = ve ^ 64;
  int vk = (lane >> 2) * 2688 + (lane & 3) * 16;
  int sx = (tbA * 128 + 32 * t4) * 896, sk = 16 * t4 * 2688 + 56 * hA - ksh;
  int l1k = t4 * 1024, l1h = t4 * 1040, l4k = t4 * 4096;
  int rk = 2 * (((hA >> 1) + ci) & 3);
#define CH(s) ((s) + rk >= 7 ? (s) + rk - 7 : (s) + rk)
  asm volatile("" : "+s"(rk));
  int k0_ = sk + rk * 172032, k1_ = sk + CH(1) * 172032, x0_ = sx + rk * 128, x1_ = sx + CH(1) * 128;
  asm volatile("" : "+s"(k0_), "+s"(k1_), "+s"(x0_), "+s"(x1_));
  asm volatile("" : "+v"(ve), "+v"(vod), "+v"(vk));
  asm volatile("" : "+s"(sx), "+s"(sk), "+s"(l1k), "+s"(l1h), "+s"(l4k));
  asm volatile("s_waitcnt lgkmcnt(0)" : "+s"(px), "+s"(pq), "+s"(pws) : : "memory");
  ST(25);
  u64 ep0;
  asm volatile("s_load_dwordx2 %0, %1, 0x0" : "=&s"(ep0) : "s"(pws) : "memory");
  const rsrc_t rqkv = RSRC(pq, DM * 3 * DM * 2), rx = RSRC(px, SEQ * DM * 2);
#define SLOT(s) (((s) & 3) * 16384)
#define WS1(s, m, so) DMA(l1h, ((m) ? L_WV : L_WK) + (s) * 4160, vk, rqkv, so, (m) ? 1792 : 896)
#define XS(s, i, so) DMA(l4k, L_X + SLOT(s) + (i) * 1024, ((i) & 1) ? vod : ve, rx, so, (i) * 8 * 896)
  if (w >= 4) { WS1(0, 0, k0_); WS1(0, 1, k0_); XS(0, 0, x0_); XS(0, 1, x0_); XS(0, 2, x0_); XS(0, 3, x0_); WS1(1, 0, k1_); WS1(1, 1, k1_); }
  int lane_ = lane, b_ = b;
  asm volatile("" : "+v"(lane_));
  asm volatile("" : "+s"(b_));
#define lane lane_
  const int r32 = lane & 31; const bool hh = lane >= 32; const int h1 = lane >> 5;
  const int q4 = (lane & 15) >> 2, p4 = lane & 3, blk = (lane >> 4) & 1, g4 = lane >> 4, c16 = lane & 15;
  const rsrc_t rs = RSRC(pws, OFF_END);
  const int l16 = lane * 16;
  unsigned ONES;
  asm volatile("s_mov_b32 %0, 0x3f803f80" : "=s"(ONES));
  const v4u sent4 = {SENT, SENT, SENT, SENT};
  unsigned tlo, thi;
#define TAG() do { asm volatile("s_waitcnt lgkmcnt(0)" : "+s"(ep0)); const u64 tag0 = ep0 + 1ull; tlo = (unsigned)tag0; thi = (unsigned)(tag0 >> 32); \
    asm volatile("" : "+s"(tlo), "+s"(thi)); } while (0)
#define TG (((tlo & 0x3fffu) | ((tlo & 0x4000u) << 1)) << 16)
#define FLAG(off) __builtin_amdgcn_raw_buffer_store_b64(v2u{tlo, thi}, rs, lane ? OFF_END : (off), 0, AUX_SC1)
  unsigned one = 1u;
  int saG = lane ? L_JUNK + c16 * 4 : L_SY + G * 48, sy = L_SY;
  PIN(one); PIN(saG); PIN(sy);
#define SECTION() \
  int bs_ = b_; KA ka = (KA)__builtin_amdgcn_kernarg_segment_ptr(); asm volatile("" : "+s"(bs_), "+s"(ka)); \
  const int xcd2 = bs_ & 7, ci2 = bs_ >> 3, ttB = 8 * xcd2 + (ci2 >> 2), cgB = ci2 & 3, nfB = 8 * cgB + 2 * t4 + G; \
  const bool actB = nfB < 28, actC = ci2 < 28; \
  const int cc = actC ? ci2 : 27, rD = (cc * 37) >> 8, tbD = 4 * (xcd2 >> 1) + rD, jj = cc - 7 * rD, eC = xcd2 & 1, tb8 = 4 * (xcd2 >> 2) + rD, slC = 7 * (xcd2 & 3) + jj, rC = (rD & 2) + (xcd2 >> 2); \
  (void)ttB; (void)nfB; (void)actB; (void)tbD; (void)eC; (void)tb8; (void)slC; (void)rC; (void)ka

  v4u wc[28], wo[16], wp[2], xr;
  int f2v;
  v2u f2;
  const int lst = t4 * 7168;
  u64 pwc;
  int rW, wcs;
#define WC_SET() do { \
  const int uD = bs_ - 224, uH = (uD * 37) >> 8; \
  rW = actC ? rC : uH & 2; \
  pwc = ((const __attribute__((address_space(4))) u64*)ka)[actC ? 4 + (t4 >> 1) : 6]; \
  wcs = actC ? (t4 & 1) * 56 * 3584 + slC * 128 : (t4 * 8 + (uH & 1) * 224) * 896 + (uD - 7 * uH) * 128; \
  asm volatile("" : "+s"(rW), "+s"(pwc), "+s"(wcs)); } while (0)
#define WC_DECL() \
  const rsrc_t rwc = RSRC(pwc, bs_ < 252 ? DM * DFF * 2 : 0); \
  const int wcv = (lane >> 3) * (actC ? 3584 : 896) + (((((lane & 7) >> 1) ^ g4) << 1) | (lane & 1)) * 16
#define WC1(r, i) DMA(lst, L_BK + (r) * 28672 + (i) * 1024, wcv, rwc, wcs + (((r) + rW) & 3) * 112 * 3584, (i) * 8 * 3584)
#define WO_DECL() \
  const rsrc_t rwo = RSRC(ka->wo, DM * DM * 2); \
  const int wov = (cgB == 3 && (q4 ^ g4) >= 2) ? NOWHERE : g4 * 896 + (q4 ^ g4) * 64 + (p4 ^ ((t4 & 1) * 2)) * 16, wox = wov ^ 32; \
  const int wos = (t4 * 28 + (G ^ ((bs_ >> 5) & 1)) * 224) * 896 + cgB * 256
#define WO1(buf, i) DMA(lst, (buf) + ((i) / 7) * 28672 + ((i) % 7) * 1024, (((i) % 7) & 1) ? wox : wov, rwo, wos, (((i) / 7) * 112 + ((i) % 7) * 4) * 896)
#define XR_LOAD() asm volatile("buffer_load_dwordx4 %0, %1, %2, %3 offen" : "=v"(xr) : "v"((16 * h1 + c16) * 896 + blk * 16), "s"(rx), "s"(ttB * 32 * 896 + (actB ? nfB : 0) * 32) : "memory")
  auto rd_wo = [&](int buf, int i0 = 0, int i1 = 16) AI {
    const int dl = 14 * h1 + 4 * blk + q4, dh = dl + 8 - blk, cc_ = buf + (p4 & 1) * 16 + (p4 >> 1) * 8;
    const int al = cc_ + dl * 256 + ((t4 ^ (dl & 3)) * 64) + (((dl >> 2) ^ G) & 1) * 32, ah = cc_ + dh * 256 + ((t4 ^ (dh & 3)) * 64) + (((dh >> 2) ^ G) & 1) * 32;
#pragma unroll
    for (int i = i0; i < i1; ++i) {
      const int x_ = (((i >> 1) ^ i) & 1) ? 0 : 32;
      v2u lo = ldtr(L, (al ^ x_) + 28 * (i >> 1) * 256), hi = ldtr(L, (ah ^ x_) + 28 * (i >> 1) * 256);
      wo[i] = v4u{lo.x, lo.y, hi.x, hi.y};
    }
  };
  auto fix_wo = [&]() AI {
#pragma unroll
    for (int i = 0; i < 16; ++i) wo[i].w &= 0xffffu;
    pin_all<0, 16>(wo);
  };
  const bool cst = p4 == 3 && blk;
  int xa[4], wa[4], wb[4];
  {
    const int row = 32 * t4 + r32, key = (row >> 1) & 7;
#pragma unroll
    for (int i = 0; i < 4; ++i) {
      xa[i] = L_X + row * 128 + (((2 * ((i + t4) & 3) + h1) ^ key) * 16);
      wa[i] = (G ? L_WV : L_WK) + ((i + t4) & 3) * 1040 + (cst ? ksh * 129 - 8 : (8 * h1 + q4) * 64 + blk * 32 + p4 * 8 + ksh); wb[i] = wa[i] + (cst ? 0 : 256);
      PIN(xa[i]); PIN(wa[i]); PIN(wb[i]);
    }
  }
  int csa = L_V + (G ? 2304 : 2816) + lane * 4, csw = csa + t4 * 4096, sqa = L_COR + t4 * 256 + lane * 4;
  PIN(csa); PIN(csw); PIN(sqa);
#define SEL_SET() do { int c_ = c16; PIN(c_); const unsigned val = (c_ & 1) ? 0x3f800000u : 0x00003f80u; const int d = c_ >> 1; \
    sel[0] = v4u{d == 0 ? val : 0u, d == 1 ? val : 0u, d == 2 ? val : 0u, d == 3 ? val : 0u}; \
    sel[1] = v4u{d == 4 ? val : 0u, d == 5 ? val : 0u, d == 6 ? val : 0u, d == 7 ? val : 0u}; PIN(sel[0]); PIN(sel[1]); } while (0)
#define WP_DECL() \
  int pa = L_WPP + (4 * h1 + q4) * 64 + blk * 32 + p4 * 8, pd = hh ? L_ZERO : pa + 1536
#define WP_READ() do { const v2u p0 = ldtr(L, pa), p1 = ldtr(L, pa + 512), p2 = ldtr(L, pa + 1024), p3 = ldtr(L, pd); \
    wp[0] = v4u{p0.x, p0.y, p1.x, p1.y}; wp[1] = v4u{p2.x, p2.y, p3.x, p3.y}; } while (0)

  if (w >= 4) { XS(1, 0, x1_); XS(1, 1, x1_); XS(1, 2, x1_); XS(1, 3, x1_); }
  { SECTION(); WC_SET(); }
  if (w < 4) {
    if (w == 3 && lane < 48) *(LAS unsigned*)(L + L_SY + lane * 4) = 0u;
    if (w < 2 && lane < 29) *(LAS v4u*)(L + (w ? L_WK : L_WV) - 16 + lane * 1040) = v4u{0x3f80u, 0u, 0x3f80u, 0u};
    if (w >= 2) {
      if ((w - 2) * 64 + lane < 98) dma(L_WPHI + (w - 2) * 1024, l16, RSRC(((KA)__builtin_amdgcn_kernarg_segment_ptr())->wphi, 16 * 28 * 28 * 2), hA * 1568 + (w - 2) * 1024);
    }
    SECTION(); WO_DECL();
    int pf3 = (ttB & 2) ? OFF_END : l16 + OFF_H + (ttB * 28 + 8 * cgB + w) * 1024;
    int pf4 = cgB < 3 ? pf3 + 4096 : OFF_END, pf5 = (w == 3 && lane < 16) ? l16 + OFF_HS + (ttB * 4 + cgB) * 256 : OFF_END;
    PIN(pf3); PIN(pf4); PIN(pf5);
    int ta0, ta1, tv, zo, po[8];
    {
      const int r0 = 8 * g4 + q4, r1 = r0 + 4;
      ta0 = L_PHI + r0 * 128 + (((4 * w + p4) ^ (r0 & 15)) * 8); ta1 = L_PHI + r1 * 128 + (((4 * w + p4) ^ (r1 & 15)) * 8);
      tv = L_V + r0 * 72 + p4 * 8; zo = ((g4 & 1) * 32 + h1 * 16 + c16) * 16 + OFF_KVP + (hA * 16 + tbA) * 4096 + w * 1024;
      PIN(ta0); PIN(ta1); PIN(tv); PIN(zo);
#pragma unroll
      for (int g = 0; g < 8; ++g) { po[g] = L_PHI + (32 * w + r32) * 128 + (((2 * g + h1) ^ (r32 & 15)) * 8); PIN(po[g]); }
    }
    WP_DECL();
    int pr_ = L_WPHI + (7 * w + (lane >> 3)) * 56 + 2 * (14 * ((lane >> 2) & 1) + 4 * p4 - (p4 == 3)), pw_ = lane < 56 ? L_WPP + w * 448 + lane * 8 : L_JUNK;
    PIN(pa); PIN(pd); PIN(pr_); PIN(pw_);
    TAG();
    unsigned tg = TG;
    asm volatile("" : "+s"(tg) : "s"(rwo), "s"(wos), "v"(wov), "v"(wox));
    {
      f32x16 acc = {};
      f32x4 c4 = {}, a2 = {};
      v4u xq[2][4];
      float cp[4], sqo, sq6;
      unsigned pe[4];
      sfor<0, 7>([&](auto S) AI {
        constexpr int s = decltype(S)::v;
        if (s == 2) VMW(0);
        if (s == 6) __builtin_amdgcn_s_setprio(2);
        if (s == 0) bar(); else { CFENCE(); __builtin_amdgcn_s_barrier(); CFENCE(); }
        if (s == 0) ST(3);
        if (s == 5) ST(41);
        if (s == 6) ST(40);
        v2u wl[4], wh[4];
        v4u (&xb)[4] = xq[s & 1];
#pragma unroll
        for (int i = 0; i < 4; ++i) { wl[i] = ldtr(L, wa[i] + s * 4160); wh[i] = ldtr(L, wb[i] + s * 4160); xb[i] = lds128(L, xa[i] + SLOT(s)); }
        if (s == 2) { for (int i = 0; i < 4; ++i) pe[i] = *(volatile LAS u16*)(L + pr_ + 2 * i); }
        if (s == 5) WP_READ();
        SCHED();
        if (s > 0) {
#pragma unroll
          for (int i = 0; i < 4; ++i) SQ4(a2, xq[(s - 1) & 1][i]);
          PIN(a2);
          if (s == 6) { sqo = HS4(a2); *(LAS float*)(L + sqa) = sqo; }
        }
        { const v4u wf = {wl[0].x, wl[0].y, wh[0].x, wh[0].y}; SUM4(c4, wf); PIN(c4); }
        if (s == 6) *(LAS float*)(L + csw) = HS4(c4);
        SCHED();
#pragma unroll
        for (int i = 0; i < 4; ++i) {
          if (s == 6 && i == 1) {
            bar();
#pragma unroll
            for (int q = 0; q < 4; ++q) cp[q] = *(LAS float*)(L + csa + q * 4096);
            sq6 = *(LAS float*)(L + sqa + 1024);
            SCHED();
          }
          const v4u wf = {wl[i].x, wl[i].y, wh[i].x, wh[i].y};
          acc = mfma32(wf, xb[i], acc);
          PIN(acc);
          if (s == 6) SCHED();
        }
        if (s == 3) *(LAS v2u*)(L + pw_) = v2u{pe[0] | (pe[1] << 16), pe[2] | ((lane & 2) ? 0u : pe[3] << 16)};
        if (s == 5) { PIN(wp[0]); PIN(wp[1]); }
      });
      unsigned hl = hilo(xsum_up(add4(cp)));
      PIN(hl);
      ST(4);
      const float nm = acc[12] * (-1.f / DM);
      acc = mfma32s(v2u{hl, hl}, hhll(nm), acc);
      SCHED();
      const float mean = upper(nm);
      float rstd = __builtin_amdgcn_rsqf(fmaxf(xsum(sqo + sq6) * (1.f / DM) - mean * mean, 0.f) + 1e-5f);
      PIN(rstd);
      SCHED();
      const f32x16 pr = feature_proj(acc, wp, hh);
      SCHED();
      ST(29);
      unsigned ep[8], en[8];
      feature_map(pr, rstd, ep, en, [](int) AI {});
#pragma unroll
      for (int g = 0; g < 4; ++g) {
        *(LAS v2u*)(L + po[g]) = v2u{ep[2 * g], ep[2 * g + 1]};
        *(LAS v2u*)(L + po[4 + g]) = v2u{en[2 * g], en[2 * g + 1]};
      }
    }
    ST(5);
    bar();
    ST(6);
    {
      v2u fa0[4], fa1[4], b00[4], b01[4], b10[4], b11[4];
      f32x4 z0 = {}, z1 = {};
#pragma unroll
      for (int s = 0; s < 6; ++s) {
        if (s < 4) {
          fa0[s] = ldtr(L, ta0 + s * 4096); fa1[s] = ldtr(L, ta1 + s * 4096);
          b00[s] = ldtr(L, tv + s * 4096); b01[s] = ldtr(L, tv + s * 4096 + 288);
          b10[s] = ldtr(L, tv + s * 4096 + 32); b11[s] = ldtr(L, tv + s * 4096 + 288 + 32);
        }
        SCHED();
        if (s >= 2) {
          const int m = s - 2;
          const v4u af_ = {fa0[m].x, fa0[m].y, fa1[m].x, fa1[m].y};
          z0 = mfma16(af_, v4u{b00[m].x, b00[m].y, b01[m].x, b01[m].y}, z0); PIN(z0);
          z1 = mfma16(af_, v4u{b10[m].x, b10[m].y, b11[m].x, b11[m].y}, z1); PIN(z1);
        }
        SCHED();
      }
      auto s0 = __builtin_amdgcn_permlane32_swap(pk2(z0.x, z0.y), pk2(z1.x, z1.y), false, false);
      auto s1 = __builtin_amdgcn_permlane32_swap(pk2(z0.z, z0.w), pk2(z1.z, z1.w), false, false);
      stc(rs, zo, 0, v4u{s0[0], s1[0], s0[1], (s1[1] & 0xffffu) | tg});
      SCHED();
    }
    ST(7);
    if (!(ttB & 2)) { stc(rs, pf3, 0, sent4); stc(rs, pf4, 0, sent4); }
    if (w == 3) stc(rs, pf5, 0, sent4);
    CFENCE();
    XR_LOAD();
    {
      v4u kp[16];
      int kso[4];
#pragma unroll
      for (int g = 0; g < 4; ++g) kso[g] = OFF_KVP + hA * 16 * 4096 + w * 1024 + ((g + tbA) & 3) * 16384;
      int kv4 = g4 * 4096 + c16 * 16, kvw = L_KV + w * 1024 + (c16 + 16 * h1) * 16 + blk * 8;
      v4u sel[2];
      SEL_SET();
      sel[0].w &= 0xffffu; sel[1].w &= 0xffffu;
      PIN(sel[0]); PIN(sel[1]); PIN(kv4); PIN(kvw);
      unsigned rank;
      VMW(1);
      {
        unsigned o_;
        asm volatile("ds_add_rtn_u32 %0, %1, %2 offset:%3\n\ts_waitcnt lgkmcnt(0)" : "=v"(o_) : "v"(saG), "v"(one), "i"(C_F2) : "memory");
        rank = __builtin_amdgcn_readfirstlane(o_);
        if (rank == 3u) FLAG(OFF_F2 + bs_ * 128);
        CFENCE();
      }
      ST(32);
      delay(KDLY);
#define KP_GRP(g) do { LDA4(kp[4 * (g)], kv4, rs, kso[g], 0); LDA4(kp[4 * (g) + 1], kv4, rs, kso[g], 256); LDA4(kp[4 * (g) + 2], kv4, rs, kso[g], 512); LDA4(kp[4 * (g) + 3], kv4, rs, kso[g], 768); } while (0)
      KP_GRP(0); KP_GRP(1); KP_GRP(2); KP_GRP(3);
      ST(8);
      WAITC(sy, 48 + C_2, 4);
      f32x4 ks_[2] = {};
      sfor<0, 4>([&](auto Gi) AI {
        constexpr int g = decltype(Gi)::v;
        VMW(12 - 4 * g); pin_all<4 * g, 4 * g + 4>(kp);
        STM((untagged<4 * g, 4 * g + 4>(kp, tg)), 10);
        if (__builtin_expect(untagged<4 * g, 4 * g + 4>(kp, tg), 0)) {
#pragma unroll 1
          for (unsigned sp = 0; sp < SPIN_CAP; ++sp) {
            __builtin_amdgcn_s_sleep(RSLP);
            KP_GRP(g); VMW(0); pin_all<4 * g, 4 * g + 4>(kp);
            if (!untagged<4 * g, 4 * g + 4>(kp, tg)) break;
          }
        }
#pragma unroll
        for (int i = 0; i < 4; ++i) ks_[i & 1] = mfma16(sel[i >> 1], kp[4 * g + 2 * (i & 1) + (i >> 1)], ks_[i & 1]);
        PIN(ks_[0]); PIN(ks_[1]);
      });
      *(LAS v2u*)(L + kvw) = v2u{pk2(ks_[0].x, ks_[0].y), pk2(ks_[0].z, ks_[0].w)};
      *(LAS v2u*)(L + kvw + 512) = v2u{pk2(ks_[1].x, ks_[1].y), pk2(ks_[1].z, ks_[1].w)};
      ST(9);
#define WOS(lo, hi) sfor<lo, hi>([&](auto N) AI { WO1(L_BV, decltype(N)::v); })
      WOS(0, 2);
      if (rank < 3u) WOS(2, 5);
      if (rank < 2u) WOS(5, 8);
      if (rank < 1u) WOS(8, 11);
      bar();
      if (rank >= 3u) WOS(2, 5);
      if (rank >= 2u) WOS(5, 8);
      if (rank >= 1u) WOS(8, 11);
      WOS(11, 14);
    }
    __builtin_amdgcn_s_setprio(0);
    PIN(xr);
#pragma unroll
    for (int i = 0; i < 16; ++i) asm volatile("" : "=v"(wo[i]));
    f2v = 0;
  } else {
#define WQ1(i) DMA(l1k, L_WQ + (i) * 4096, vk, rqkv, sk + CH(i) * 172032, 0)
    v4u xf[28], phq[4];
    f32x16 acc = {};
    f32x4 c4v = {};
    v2u wl[4], wh[4];
    float cp[4];
    __builtin_amdgcn_s_setprio(1);
#define RQ(s, j) do { if ((s) < 5) { if ((j) < 2) WS1((s) + 2, (j) & 1, sk + CH((s) + 2) * 172032); else if ((j) < 6) XS((s) + 2, ((j) + 2) & 3, sx + CH((s) + 2) * 128); } \
      else if ((j) < NQ5) WQ1(j); } while (0)
    auto round = [&](auto S) AI {
      constexpr int s = decltype(S)::v;
      VMW(s < 6 ? 6 : NQ5);
      if (s == 0) ST(3);
      if (s == 3) ST(28);
      if (s == 5) ST(41);
      if (s == 4) ST(2);
      if (s == 6) ST(4);
      __builtin_amdgcn_s_barrier(); CFENCE();
#pragma unroll
      for (int i = 0; i < 4; ++i) { wl[i] = ldtr(L, wa[i] + s * 4160); wh[i] = ldtr(L, wb[i] + s * 4160); xf[4 * s + i] = lds128(L, xa[i] + SLOT(s)); }
      SCHED();
      if constexpr (s < 6) {
        sfor<0, 4>([&](auto J) AI { RQ(s, decltype(J)::v); });
        SCHED();
        sfor<0, 4>([&](auto I) AI {
          constexpr int i = decltype(I)::v;
          const v4u wf = {wl[i].x, wl[i].y, wh[i].x, wh[i].y};
          acc = mfma32(wf, xf[4 * s + i], acc); PIN(acc);
          if constexpr (i < 3) RQ(s, 4 + i);
          SCHED();
        });
      }
      { const v4u w0 = {wl[0].x, wl[0].y, wh[0].x, wh[0].y}; SUM4(c4v, w0); PIN(c4v); }
      SCHED();
    };
    round(IC<0>{});
    SECTION(); WO_DECL();
    TAG();
    asm volatile("" ::"s"(rwo), "s"(wos), "v"(wov), "v"(wox));
    sfor<1, 7>(round);
    *(LAS float*)(L + csw) = HS4(c4v);
    float sq6;
    { f32x4 a2 = {}; SQ4(a2, xf[24]); SQ4(a2, xf[25]); SQ4(a2, xf[26]); SQ4(a2, xf[27]); sq6 = HS4(a2); *(LAS float*)(L + sqa + 1024) = sq6; }
    bar();
    ST(40);
#pragma unroll
    for (int i = 0; i < 4; ++i) cp[i] = *(LAS float*)(L + csa + i * 4096);
    const float sq5 = *(LAS float*)(L + sqa);
    constexpr int NTA = 7 - NQ5 + 14;
#define TR(j) do { if ((j) < 7 - NQ5) WQ1((NQ5 + (j)) % 7); else WO1(L_BK, ((j) - 7 + NQ5 + 14) % 14); } while (0)
#define TQ(j) do { if ((j) < NTQ) TR(j); SCHED(); } while (0)
    TQ(0); TQ(1);
    sfor<0, 4>([&](auto I) AI {
      constexpr int i = decltype(I)::v;
      const v4u wf = {wl[i].x, wl[i].y, wh[i].x, wh[i].y};
      acc = mfma32(wf, xf[24 + i], acc); PIN(acc);
      TQ(2 + i);
    });
    ST(5);
    v2u mfr;
    float rstd;
    {
      unsigned cx = hilo(xsum_up(add4(cp)));
      PIN(cx);
      TQ(6);
      float nm = acc[12] * (-1.f / DM);
      mfr = hhll(nm);
      acc = mfma32s(v2u{cx, cx}, mfr, acc);
      PIN(acc); PIN(nm);
      TQ(7); TQ(8);
      float mean = upper(nm);
      PIN(mean);
      TQ(9);
      rstd = __builtin_amdgcn_rsqf(fmaxf(xsum(sq5 + sq6) * (1.f / DM) - mean * mean, 0.f) + 1e-5f);
      PIN(rstd);
      TQ(10);
    }
    acc *= rstd;
    if (hh) acc[12] = 1.0f;
    {
      const int ro = L_V + t4 * 4096 + r32 * 72 + h1 * 8;
#pragma unroll
      for (int g = 0; g < 4; ++g) *(LAS v2u*)(L + ro + g * 16) = v2u{pk2(acc[4 * g], acc[4 * g + 1]), pk2(acc[4 * g + 2], acc[4 * g + 3])};
    }
    ST(7);
    __builtin_amdgcn_s_setprio(2);
    VMW(NTQ - 7 + NQ5);
    bar();
    sfor<NTQ, NTA>([&](auto J) AI { TR(decltype(J)::v); });
    {
      constexpr int P = 5;
      int qa[4];
#pragma unroll
      for (int i = 0; i < 4; ++i) { qa[i] = L_WQ + (8 * h1 + q4) * 64 + blk * 32 + p4 * 8 + ksh + ((i + t4) & 3) * 1024; PIN(qa[i]); }
      v2u bl[P], bh[P];
      f32x4 c4 = {};
      unsigned cx = 0u;
      acc = f32x16{};
      ST(6);
#pragma unroll
      for (int i = 0; i < P; ++i) { bl[i] = ldtr(L, qa[0] + i * 4096); bh[i] = ldtr(L, qa[0] + i * 4096 + 256); }
      SCHED();
      sfor<0, 28>([&](auto I) AI {
        constexpr int n = decltype(I)::v, m = n + P;
        const v4u wf = {bl[n % P].x, bl[n % P].y, bh[n % P].x, bh[n % P].y};
        acc = mfma32(wf, xf[4 * (n % 7) + n / 7], acc);
        PIN(acc);
        if constexpr (n < 7) { SUM4(c4, wf); PIN(c4); }
        if constexpr (m < 28) { bl[n % P] = ldtr(L, qa[m / 7] + (m % 7) * 4096); bh[n % P] = ldtr(L, qa[m / 7] + (m % 7) * 4096 + 256); }
        if constexpr (n == 7) *(LAS float*)(L + csw + 256) = HS4(c4);
        if constexpr (n == 20) { VMW(0); INC(saG, C_1); }
        if constexpr (n == 23) INC(saG, C_2);
        if constexpr (n == 24) {
          WAITC(sy, 48 + C_1, 4);
#pragma unroll
          for (int i = 0; i < 4; ++i) cp[i] = *(LAS float*)(L + csa + i * 4096 + 256);
        }
        if constexpr (n == 25) { WP_DECL(); WP_READ(); }
        if constexpr (n == 27) { cx = hilo(xsum_up(add4(cp))); PIN(cx); }
        SCHED();
      });
      acc = mfma32s(v2u{cx, cx}, mfr, acc);
      PIN(acc);
    }
    ST(34);
    rd_wo(L_BK, 0, 4);
    SCHED();
    {
      unsigned ep[8], en[8];
      const f32x16 pr = feature_proj(acc, wp, hh);
      SCHED();
      rd_wo(L_BK, 4, 8);
      feature_map(pr, rstd, ep, en, [&](int i) AI { if (8 + 2 * i < 16) { rd_wo(L_BK, 8 + 2 * i, 8 + 2 * i + 2); SCHED(); } });
      phq[0] = v4u{ep[0], ep[1], ep[2], ep[3]}; phq[1] = v4u{ep[4], ep[5], ep[6], ep[7]}; phq[2] = v4u{en[0], en[1], en[2], en[3]}; phq[3] = v4u{en[4], en[5], en[6], en[7]};
      pin_all<0, 4>(phq);
    }
    ST(8);
    f2v = (tb8 + 8 * (8 * t4 + (lane & 7))) * 128;
    pin_all<0, 16>(wo);
    WC_DECL();
    int oo = l16 + OFF_O + ((tbA * 4 + t4) * 16 + hA) * 2048, kva;
    {
      const int g_ = r32 >> 2;
      kva = L_KV + h1 * 512 + ((q4 & 2) && p4 == 3 ? (g_ == 7 ? 28 : 29) : 14 * blk + 4 * q4 - (q4 == 3) + p4) * 16;
    }
    unsigned tgv = TG;
    asm volatile("" : "+s"(tgv));
    PIN(oo); PIN(f2v); PIN(kva);
    asm volatile("" ::"s"(rwc), "s"(wcs), "v"(wcv));
    ST(33);
    __builtin_amdgcn_s_setprio(3);
    f32x16 o = {};
    {
      v4u kf[4];
      bar();
#pragma unroll
      for (int s = 0; s < 4; ++s) kf[s] = lds128(L, kva + s * 1024);
      SCHED();
      ST(9);
#pragma unroll
      for (int s = 0; s < 4; ++s) o = mfma32(kf[s], phq[s], o);
    }
    o *= __builtin_amdgcn_rcpf(upper(o[15]) + 1e-6f);
    v4u o0 = pack8(o, 0), o1 = pack8(o, 1);
    o0.w = (o0.w & 0xffffu) | tgv; o1.w = (o1.w & 0xffffu) | tgv;
    stc(rs, oo, 0, o0); stc(rs, oo, 1024, o1);
    SCHED();
    __builtin_amdgcn_s_setprio(2);
    ST(30);
    XR_LOAD();
#pragma unroll
    for (int i = 0; i < 7; ++i) WC1(0, i);
  }
  int t4c = t4;
  asm volatile("" : "+s"(t4c));
#define t4 t4c
  SECTION(); WC_DECL();
  const int uD = bs_ - 224, uH = (uD * 37) >> 8;
  int wcl = lst + G * 28672, wcg = wcs + ((2 + G + rC) & 3) * 112 * 3584;
  const rsrc_t rwc2 = RSRC(pwc, actC ? DM * DFF * 2 : 0);
  asm volatile("" : "+s"(wcl), "+s"(wcg) : "s"(rwc), "s"(rwc2), "v"(wcv));
  int rbG = L_SY + G * 48, rb2 = L_SY + (lane & 1) * 48;
  const int GH = G ^ ((bs_ >> 5) & 1);
  int oi = g4 * 512 + c16 * 16 + (GH ? L_OV : L_OK), pw = l16 + L_XCH + w * 1024, po = pw ^ 4096, ho = l16 + (actB ? OFF_H + (ttB * 28 + nfB) * 1024 : OFF_END);
  int og = OFF_O + ttB * 32768 + t4 * 8192, os = L_OK + t4 * 8192 + (t4 >> 1) * (L_OV - 16384 - L_OK);
  asm volatile("" : "+s"(og), "+s"(os));
  int ol = os + l16;
  int wca = L_BK + h1 * 2048 + (4 * blk + q4) * 128 + ((t4 ^ ((q4 >> 1) | (blk << 1))) * 32) + p4 * 8, wcb = wca + h1 * 14336;
#define RD_WC(n, a, b) do { const int o_ = ((((n) % 14) & ~1) / 7) * 28672 + ((((n) % 14) & ~1) % 7) * 2048 + ((n) & 1) * 14336, ad_ = ((n) % 14) / 2 == 3 ? (b) : (a); \
    const v2u lo = ldtr(L, ad_ + o_), hi = ldtr(L, ad_ + o_ + 1024); wc[n] = v4u{lo.x, lo.y, hi.x, hi.y}; } while (0)
  int ao = l16 + OFF_ACT + (actC ? (tb8 * 8 + 4 * G) * 112 + 4 * slC + t4 : ((uD >> 1) * 4 + 2) * 112 + (uD & 1) * 56 + 7 * w) * 1024;
  PIN(rbG); PIN(rb2); PIN(oi); PIN(pw); PIN(po); PIN(ho); PIN(ol); PIN(wca); PIN(wcb); PIN(ao);
  int fdv = (jj + 7 * ((w & 1) + 2 * (w >> 2))) * 128, fps = tb8 * 4096 + (28 + (slC >= 14)) * 128 + G * 64;
  asm volatile("" : "+s"(fps));
  const int tt0 = tb8 * 8 + 4 * G, gb = G ? L_BV : L_BK;
  int hso = actC ? OFF_H + tt0 * 28672 + ((t4 + rC) & 3) * 7168 : OFF_END, hld = lst + gb;
  asm volatile("" : "+s"(hso), "+s"(hld));
  int hss = OFF_HS + (tt0 + t4) * 1024, f3s = OFF_F3 + (tb8 * 32 + slC) * 128 + G * 64, f3b = (tbD >> 1) * 4096 + (tbD & 1) * 64;
  u64 pout = (u64)ka->out;
  asm volatile("" : "+s"(hss), "+s"(f3s), "+s"(f3b), "+s"(pout));
  PIN(fdv);
  if (G) {
    VMW(7);
    ST(31);
    delay(ODLY);
    GATHER(8, os, 0, og, 0);
    INC(saG, C_3);
    ST(11);
  }
  const int wdo = OFF_WD + 2 * jj * 112 * 1024;
  int rot[7];
#pragma unroll
  for (int p = 0; p < 7; ++p) { int r_ = p + jj; rot[p] = (r_ >= 7 ? r_ - 7 : r_) * 2048 + 14 * w * 1024 + wdo; }
  int wdl = w < 5 ? 57344 + w * 5120 : 143360 + (w - 5) * 5120;
  int aoE = OFF_ACT + tbD * 4 * 114688 + eC * 256 - wdo;
  asm volatile("" : "+s"(rot[0]), "+s"(rot[1]), "+s"(rot[2]), "+s"(rot[3]), "+s"(rot[4]), "+s"(rot[5]), "+s"(rot[6]), "+s"(wdl), "+s"(aoE));
  __builtin_amdgcn_s_setprio(1);
  {
    f32x4 aP[2], aO[2];
    PIN(xr);
    {
      auto x0 = __builtin_amdgcn_permlane32_swap(xr.x, xr.z, false, false);
      auto x1 = __builtin_amdgcn_permlane32_swap(xr.y, xr.w, false, false);
      aO[0] = f32x4{lo_f(x0[0]), hi_f(x0[0]), lo_f(x1[0]), hi_f(x1[0])};
      aO[1] = f32x4{lo_f(x0[1]), hi_f(x0[1]), lo_f(x1[1]), hi_f(x1[1])};
      PIN(aO[0]); PIN(aO[1]);
    }
    if (G) {
      const unsigned tg = TG;
      LDA2(f2, f2v, rs, OFF_F2);
      fix_wo();
      VMW(5);
      ST(37);
      unsigned m = lds_tag<0, 4>(L, ol, tg);
      PIN(m);
      VMW(1);
      ST(38);
      m |= lds_tag<4, 8>(L, ol, tg);
      STM(__any(m > 0xffffu), 10);
      if (__builtin_expect(__any(m > 0xffffu), 0)) {
#pragma unroll 1
        for (unsigned sp = 0; sp < SPIN_CAP; ++sp) {
          __builtin_amdgcn_s_sleep(RSLP);
          GATHER(8, os, 0, og, 0); VMW(0);
          if (!__any((lds_tag<0, 8>(L, ol, tg)) > 0xffffu)) break;
        }
      }
    } else {
      ST(37);
      f2 = v2u{tlo, thi};
      VMW(0); INC(saG, C_1);
      WAITC(rbG, C_1, 4);
      ST(33);
      rd_wo(L_BV);
      fix_wo();
      WAITC(rbG, 48 + C_3, 4);
      ST(34);
#pragma unroll
      for (int i = 0; i < 7; ++i) WC1(1, i);
      ST(38);
    }
    bar();
    ST(12);
    __builtin_amdgcn_s_setprio(3);
    constexpr int NOB = 6, NI = 6;
    v4u ob[16], eh;
#pragma unroll
    for (int i = 0; i < NOB; ++i) ob[i] = lds128(L, oi + (i >> 1) * 2048 + (i & 1) * 256);
    SCHED();
#pragma unroll
    for (int i = 0; i < 32; ++i) {
      const bool own = i < 2 * NI ? (i >> 1) & 1 : i >= 16 + NI;
      const int f = i < 2 * NI ? (i >> 2) * 2 + (i & 1) : own ? i - 16 : i - NI;
      if (own) { aO[f & 1] = mfma16(wo[f | 1], ob[f], aO[f & 1]); PIN(aO[f & 1]); }
      else { aP[f & 1] = mfma16(wo[f & ~1], ob[f], f < 2 ? f32x4{} : aP[f & 1]); PIN(aP[f & 1]); }
      FAIR(i, 5, 11, 17);
      if (i + NOB < 16) ob[i + NOB] = lds128(L, oi + ((i + NOB) >> 1) * 2048 + ((i + NOB) & 1) * 256);
      if (i == 17 + NI) *(LAS v4u*)(L + pw) = v4u{pk2(aP[0].x, aP[0].y), pk2(aP[0].z, aP[0].w), pk2(aP[1].x, aP[1].y), pk2(aP[1].z, aP[1].w)};
      if (i == 21 + NI) { bar(); eh = lds128(L, po); }
      if (i >= 12 && i < 18) RD_WC(i - 12, wca, wcb);
      if (i > 4 && (i & 3) == 1) DMA(wcl, L_BV + (i / 4 - 1) * 1024, wcv, rwc2, wcg, (i / 4 - 1) * 8 * 3584);
      SCHED();
    }
    v4u hv;
    {
      aO[0] += f32x4{lo_f(eh.x), hi_f(eh.x), lo_f(eh.y), hi_f(eh.y)}; aO[1] += f32x4{lo_f(eh.z), hi_f(eh.z), lo_f(eh.w), hi_f(eh.w)};
      auto y0 = __builtin_amdgcn_permlane16_swap(guard(pk2(aO[0].x, aO[0].y)), guard(pk2(aO[1].x, aO[1].y)), false, false);
      auto y1 = __builtin_amdgcn_permlane16_swap(pk2(aO[0].z, aO[0].w), pk2(aO[1].z, aO[1].w), false, false);
      hv = v4u{y0[0], y1[0], y0[1], y1[1]};
    }
    stc(rs, ho, 0, hv);
    SCHED();
    ST(13);
    if (actC) VMW(1);
    __builtin_amdgcn_s_barrier(); CFENCE();
#pragma unroll
    for (int i = 6; i < 14; ++i) RD_WC(i, wca, wcb);
    SCHED();
    {
      f32x4 a1_ = {}, a2_ = {};
      SUM4(a1_, hv); SQ4(a2_, hv);
      *(LAS f32x2*)(L + L_ST2 + r32 * 144 + h1 * 64 + (2 * t4 + G) * 8) = f32x2{HS4(a1_), HS4(a2_)};
    }
    SCHED();
    {
      int a1 = wca - L_BK + L_BV, b1 = wcb - L_BK + L_BV;
      PIN(a1); PIN(b1);
#pragma unroll
      for (int i = 14; i < 28; ++i) RD_WC(i, a1, b1);
    }
  }
  VMW(0); PIN(f2);
  flag_ensure(f2, rs, f2v, OFF_F2, tlo, thi);
  bar();
  ST(39);
  if (w == 0) {
    const int so = L_ST2 + r32 * 144 + h1 * 64;
    f32x4 p0 = *(LAS f32x4*)(L + so), p1 = *(LAS f32x4*)(L + so + 16), p2 = *(LAS f32x4*)(L + so + 32), p3 = *(LAS f32x4*)(L + so + 48);
    p0 += p1;
    if (cgB < 3) p0 += p2 + p3;
    float s1 = xsum(p0.x + p0.z), s2 = xsum(p0.y + p0.w);
    __builtin_amdgcn_raw_buffer_store_b64(v2u{guard(__float_as_uint(s1)), __float_as_uint(s2)}, rs, hh ? OFF_END : OFF_HS + (ttB * 4 + cgB) * 256 + r32 * 8, 0, AUX_SC1);
  }
  if (!actC) {
    if (bs_ == 255 && w == 4) FLAG(OFF_EPOCH);
    if (uD < 28) {
      const int a0 = L_BK + G * 28672 + ((w & 2) + h1) * 7168 + q4 * 128 + (((w & 1) * 2 + blk) ^ (q4 >> 1)) * 32 + p4 * 8, a1 = a0 ^ 64;
      const int o0 = l16 + OFF_WD + ((2 * (uD - 7 * uH) + (w & 1)) * 112 + (rW + G) * 28 + (uH & 1) * 14 + ((w >> 1) & 1)) * 1024;
#pragma unroll
      for (int i = 0; i < 7; ++i) {
        const v2u lo = ldtr(L, a0 + i * 1024), hi = ldtr(L, a1 + i * 1024 + 512);
        stc(rs, o0, i * 2048, v4u{lo.x, lo.y, hi.x, hi.y});
      }
      VMW(0); bar();
      if (w == 0) FLAG(OFF_FD + uD * 128);
      ST(21);
    }
#pragma unroll
    for (int i = 0; i < 14; ++i) stc(rs, ao, (i / 7) * 114688 + (i % 7) * 1024, sent4);
    VMW(0); bar();
    if (w == 0) FLAG(OFF_F3 + (uD >> 2) * 4096 + (28 + (uD & 1)) * 128 + (uD & 2) * 32);
    ST(22);
  } else {
    CFENCE();
    f32x4 c4c = {};
    GATHER(3, hld, 0, hso, 0);
#define GATHER_CS(o) do { _Pragma("unroll") for (int i = 0; i < 7; ++i) { \
      if (i < 4) DMAC(hld, (3 + i) * 1024, l16, rs, hso, (3 + i) * 1024); \
      SUM4(c4c, wc[(o) + 4 * i]); SUM4(c4c, wc[(o) + 4 * i + 2]); PIN(c4c); SCHED(); } } while (0)
    if (G) GATHER_CS(1); else GATHER_CS(0);
    ST(14);
    int lcor = (G ? L_COR2 : L_COR) + c16 * 32, lcw = lcor + t4 * 512 + h1 * 16 + blk * 8;
    int hlo = t4 * 7168 + l16 + gb;
    int lq = h1 * 1024 + blk * 512 + c16 * 16;
    int lgq = lq + gb;
    PIN(lq); PIN(lgq);
#define HA(t, ks) (lgq + ((t) & 1) * 28672 + ((ks) >> 1) * 2048 + ((ks) & 1) * 256)
    v4u wd[28];
#define WD_LD(n, va) LDA4(wd[n], va, rs, rot[((n) % 14) >> 1], ((n) & 1) * 256)
    { const unsigned hl = hilo(xsum16(xsum(HS4(c4c)))); *(LAS unsigned*)(L + L_CS + w * 128 + r32 * 4) = blk ? 0u : hl; }
    int hsv = r32 * 8 + h1 * 512, hsw = hsv + 256, csr = L_CS + t4 * 128 + (g4 ? 64 : c16 * 4);
    PIN(csr); PIN(hsv); PIN(hsw); PIN(fdv); PIN(hlo); PIN(lcor); PIN(lcw);
    GATHER(H1A, hld, 28672, hso, 28672);
    int f3v; { int j = (14 * w) / 4 + (lane < 4 ? lane : 4); j = j < 27 ? j : 27; j = lane == 5 ? 28 + G : j; f3v = lane == 6 ? fps : j * 128 + f3b; }
    int f3o = (lane | t4) ? OFF_END : f3s;
    PIN(f3v); PIN(f3o);
    ST(36);
    VMW(H1A);
    ST(35);
    {
      const unsigned m = lds_max<0, 7>(L, hlo);
      STM(__any(m == SENT), 26);
      if (__builtin_expect(__any(m == SENT), 0)) REGATHER(7, hld, hlo, 0, hso, 0);
    }
    bar();
    ST(15);
    Acc accA, accB;
    v4u buf[4], cm, cr;
    v2u csf[2], fd;
#define C_STEP(A, ks, B) do { \
      A.a[0][(ks) & 1] = mfma16(wc[(ks) & ~1], B, (ks) < 2 ? f32x4{} : A.a[0][(ks) & 1]); PIN(A.a[0][(ks) & 1]); \
      A.a[1][(ks) & 1] = mfma16(wc[(ks) | 1], B, (ks) < 2 ? f32x4{} : A.a[1][(ks) & 1]); PIN(A.a[1][(ks) & 1]); } while (0)
#define C_FIX(A) do { asm volatile("s_nop 3" : "+v"(A.a[0][0]), "+v"(A.a[1][0]), "+v"(A.a[0][1]), "+v"(A.a[1][1])); SCHED(); _Pragma("unroll") for (int q = 0; q < 4; ++q) { \
      A.a[q & 1][q >> 1] = mfma16s(csf[q & 1], (q >> 1) ? v2u{cm.z, cm.w} : v2u{cm.x, cm.y}, A.a[q & 1][q >> 1]); PIN(A.a[q & 1][q >> 1]); SCHED(); } } while (0)
#define C_NEXT(t, ks, W) do { if ((ks) + 4 < 28) buf[(ks) % 4] = lds128(L, HA(t, (ks) + 4)); \
      else if ((ks) == (W)) { _Pragma("unroll") for (int q = 24; q <= (W); ++q) buf[q % 4] = lds128(L, HA((t) + 1, q - 24)); } \
      else if ((ks) > (W)) buf[(ks) % 4] = lds128(L, HA((t) + 1, (ks) - 24)); } while (0)
    {
      v2u q0, q1, hl2;
      float mean2, var2;
      unsigned cs0, cs1, m1;
      v4u ck[4];
#pragma unroll
      for (int i = 0; i < 4; ++i) buf[i] = lds128(L, HA(0, i));
      SCHED();
#pragma unroll
      for (int ks = 0; ks < 28; ++ks) {
        C_STEP(accA, ks, buf[ks % 4]);
        if (ks == 26) { WAITC(rbG, C_H + 4, 4); cm = lds128(L, lcor); cr = lds128(L, lcor + 16); }
        C_NEXT(0, ks, 26);
        if (ks == 8) LDA2(q0, hsv, rs, hss);
        if (ks == 9) LDA2(q1, hsw, rs, hss);
        if (H1A + ks < 7) DMAC(hld, 28672 + (H1A + ks) * 1024, l16, rs, hso, 28672 + (H1A + ks) * 1024);
        if (ks == 11) { cs0 = *(LAS unsigned*)(L + csr); cs1 = *(LAS unsigned*)(L + csr + 512); }
        if (ks == 14) { csf[0] = v2u{cs0, cs0}; csf[1] = v2u{cs1, cs1}; PIN(csf[0]); PIN(csf[1]); }
        if (ks == 16) { VMW(2); ck[0] = lds128(L, hlo + 28672); ck[1] = lds128(L, hlo + 29696); ck[2] = lds128(L, hlo + 30720); ck[3] = lds128(L, hlo + 31744); }
        if (ks == 18) {
          m1 = mx4(mx4(mx4(max(ck[0].x, ck[0].z), ck[1]), ck[2]), ck[3]); PIN(m1);
          ck[0] = lds128(L, hlo + 32768); ck[1] = lds128(L, hlo + 33792); ck[2] = lds128(L, hlo + 34816);
        }
        if (ks == 20) {
          m1 = mx4(mx4(mx4(m1, ck[0]), ck[1]), ck[2]);
          STM(__any(m1 == SENT), 26);
          if (__builtin_expect(__any(m1 == SENT), 0)) REGATHER(7, hld, hlo, 28672, hso, 28672);
          VMW(0); PIN(q0); PIN(q1);
          LDA2(fd, fdv, rs, OFF_FD);
          if (__builtin_expect(__any(max(q0.x, q1.x) == SENT), 0)) {
#pragma unroll 1
            for (unsigned sp = 0; sp < SPIN_CAP; ++sp) {
              __builtin_amdgcn_s_sleep(RSLP);
              CFENCE();
              q0 = ldc2(rs, hsv, hss); q1 = ldc2(rs, hsw, hss);
              if (!__any(max(q0.x, q1.x) == SENT)) break;
            }
          }
          mean2 = xsum(__uint_as_float(q0.x) + __uint_as_float(q1.x)) * (1.f / DM); PIN(mean2);
        }
        if (ks == 21) { var2 = fmaxf(xsum(__uint_as_float(q0.y) + __uint_as_float(q1.y)) * (1.f / DM) - mean2 * mean2, 0.f) + 1e-5f; PIN(var2); }
        if (ks == 22) { hl2 = hhll(-mean2); PIN(hl2); }
        if (ks == 23) {
          *(LAS v2u*)(L + lcw) = hh ? v2u{__float_as_uint(__builtin_amdgcn_rsqf(var2)), __float_as_uint(var2)} : hl2;
          INC(saG, C_H + 4);
        }
        if (ks == 21 || ks == 22) stc(rs, ao, (22 - ks) * 114688, sent4);
        SCHED();
      }
    }
    ST(16);
    float k1[2], k2[2], vr[2], tz[8], tn[8]; unsigned ow[4];
    C_FIX(accA);
    auto epi_consts = [&]() AI {
#pragma unroll
      for (int th = 0; th < 2; ++th) {
        const float rr = __uint_as_float(th ? cr.z : cr.x);
        vr[th] = __uint_as_float(th ? cr.w : cr.y);
        k1[th] = (-2.f * 0.7978845608028654f * L2E) * rr; k2[th] = k1[th] * 0.044715f * rr * rr;
      }
    };
    auto epi = [&](const Acc& pv, int s) AI {
      const int a = s / 6 * 2, b = a + 1, ph = s % 6, th = a >> 2;
      const float ga = pv.a[0][th][a & 3], gb_ = pv.a[0][th][b & 3], ua = pv.a[1][th][a & 3], ub = pv.a[1][th][b & 3], c1 = k1[th], c2 = k2[th], cv = vr[th];
      if (ph == 0) { tz[a] = ga * ga; PIN(tz[a]); tz[b] = gb_ * gb_; PIN(tz[b]); tz[a] = __builtin_fmaf(c2, tz[a], c1); PIN(tz[a]); }
      else if (ph == 1) { tz[b] = __builtin_fmaf(c2, tz[b], c1); PIN(tz[b]); tz[a] *= ga; PIN(tz[a]); tz[b] *= gb_; PIN(tz[b]); }
      else if (ph == 2) { tz[a] = __builtin_amdgcn_exp2f(tz[a]); PIN(tz[a]); tn[a] = ga * ua; PIN(tn[a]); tz[b] = __builtin_amdgcn_exp2f(tz[b]); PIN(tz[b]); }
      else if (ph == 3) { tn[b] = gb_ * ub; PIN(tn[b]); tz[a] = __builtin_fmaf(tz[a], cv, cv); PIN(tz[a]); tz[b] = __builtin_fmaf(tz[b], cv, cv); PIN(tz[b]); }
      else if (ph == 4) { tz[a] = __builtin_amdgcn_rcpf(tz[a]); PIN(tz[a]); tz[b] = __builtin_amdgcn_rcpf(tz[b]); PIN(tz[b]); if (a & 2) { ow[a / 2 - 1] = guard(ow[a / 2 - 1]); PIN(ow[a / 2 - 1]); } }
      else { tn[a] *= tz[a]; PIN(tn[a]); tn[b] *= tz[b]; PIN(tn[b]); ow[a >> 1] = pk2(tn[a], tn[b]); PIN(ow[a >> 1]); }
    };
    auto act_frag = [&]() AI -> v4u {
      auto x0 = __builtin_amdgcn_permlane16_swap(ow[0], ow[2], false, false);
      auto x1 = __builtin_amdgcn_permlane16_swap(ow[1], ow[3], false, false);
      return v4u{x0[0], x1[0], x0[1], x1[1]};
    };
    v2u f3;
    int lqb, av[4];
    rsrc_t rsD = rs;
    v4u af[28];
#define AF_LD(n) do { const int v_ = av[(n) / 7], s_ = rot[(n) % 7]; const rsrc_t r_ = rsD; v4u t_; LDA4(t_, v_, r_, s_, 0); af[n] = t_; } while (0)
#define C_TILE(t, CUR, PRV, S, W) do { \
      v4u crn; \
      epi_consts(); \
      SCHED(); \
      _Pragma("unroll") for (int ks = 0; ks < 28; ++ks) { \
        C_STEP(CUR, ks, buf[ks % 4]); \
        if ((t) < 3 && ks == (W)) WAITC(rbG, C_H + ((t) + 1) * 4, 4); \
        if (ks == 25) { cm = lds128(L, lcor + (t) * 512); crn = lds128(L, lcor + (t) * 512 + 16); } \
        if ((t) < 3 && ks < 7) DMAC(hld, (((t) + 1) & 1) * 28672 + ks * 1024, l16, rs, hso, ((t) + 1) * 28672 + ks * 1024); \
        if ((t) == 1 && ks == 9) { VMW(9); PIN(fd); flag_ensure(fd, rs, fdv, OFF_FD, tlo, thi); }     \
        if ((t) == 3 && ks == 17) { VMW(6); PIN(f3); flag_ensure(f3, rs, f3v, OFF_F3, tlo, thi); }     \
        if ((t) == 1 && ks == 25) __builtin_amdgcn_raw_buffer_store_b64(v2u{tlo, thi}, rs, f3o, 0, AUX_SC1);     \
        C_NEXT(t, ks, (t) == 3 ? 99 : (W)); \
        if ((t) < 3 && ks == (S)) { VMW(((S) - 9) / 2); INC(saG, C_H + ((t) + 1) * 4); } \
        if (ks >= 2 && ks < 26) epi(PRV, ks - 2); \
        if (ks == 26) stc(rs, ao, ((t) - 1) * 114688, act_frag()); \
        if ((t) < 3 && ks >= 10 && (ks & 1) == 0) { const int q_ = ((t) - 1) * 9 + (ks - 10) / 2; \
          if (q_ < NPRE) WD_LD(q_ < NPRE ? q_ : 0, lq); \
          else { const int n_ = 28 - NLDS + q_ - NPRE; DMAC(wdl, (q_ - NPRE) * 1024, lq, rs, rot[(n_ % 14) >> 1], (n_ & 1) * 256 + 114688); } } \
        if ((t) == 3 && ks == 4) LDA2(f3, f3v, rs, OFF_F3); \
        if ((t) == 3 && ks >= 20 && !(ks & 1)) AF_LD(ks >= 20 ? (ks - 20) / 2 : 0); \
        if ((t) == 3 && (ks & 1) == 1 && NPRE + (ks >> 1) < 28 - NLDS) WD_LD(NPRE + (ks >> 1), NPRE + (ks >> 1) < 14 ? lq : lqb); \
        SCHED(); \
      } \
      C_FIX(CUR); cr = crn; } while (0)
    static_assert(NPRE + NLDS == 18 && NPRE <= 14, "");
    if (G) __builtin_amdgcn_s_setprio(1);
    C_TILE(1, accB, accA, 23, 25);
    C_TILE(2, accA, accB, 21, 23);
    ST(18);
    __builtin_amdgcn_s_setprio(0);
    lqb = lq + 114688;
    av[0] = lq + aoE;
    PIN(lqb); PIN(av[0]);
    asm volatile("" : "+s"(rsD));
    C_TILE(3, accB, accA, 99, 99);
    ST(19);
    epi_consts();
#pragma unroll
    for (int s = 0; s < 24; ++s) {
      if (s / 4 != 4 || !(s & 1)) epi(accB, (s & 3) * 6 + s / 4);
      else { const int a = (s & 3) * 2; tz[a] = __builtin_amdgcn_rcpf(tz[a]); tz[a + 1] = __builtin_amdgcn_rcpf(tz[a + 1]); }
      if (s == 5 || s == 11) { AF_LD(NE3 + s / 6); SCHED(); }
    }
    ow[0] = guard(ow[0]); ow[2] = guard(ow[2]);
    stc(rs, ao, 3 * 114688, act_frag());
    SCHED();
    ST(20);
#pragma unroll
    for (int t = 1; t < 4; ++t) { av[t] = av[0] + t * 114688; PIN(av[t]); }
    const rsrc_t ro_ = RSRC(pout, SEQ * DM * 2);
    const int tR = tbD * 4 + 2 * G;
    const int tok = 16 * eC + 4 * t4 + p4;
    int ov = ((tR + blk) * 32 + tok) * 896 + 128 * jj + 32 * q4 + 16 * h1, rw[4];
#pragma unroll
    for (int cb = 0; cb < 4; ++cb) { rw[cb] = L_RED + w * 4096 + cb * 1024 + (l16 ^ (cb * 64)); PIN(rw[cb]); }
    v4u sel[2];
    v2u hr[2];
    int hv_ = blk * 28672 + q4 * 1024 + tok * 16 + h1 * 8, ra = L_RED + G * 32768 + g4 * 4096 + q4 * 1024 + ((t4 ^ q4) * 4 + p4) * 16;
    {
      SEL_SET();
      PIN(ov); PIN(hv_); PIN(ra);
      asm volatile("" ::"s"(ro_));
    }
#pragma unroll
    for (int n = 0; n < NLDS; ++n) wd[28 - NLDS + n] = lds128(L, wdl + n * 1024 + l16);
    INC(saG, C_E);
    ST(21);
    __builtin_amdgcn_s_setprio(3);
    f32x4 s0, s1;
    {
      f32x4 dd[4][2] = {};
#define DM_(cb, i) do { constexpr int m_ = n + (i), e_ = (m_ / 7) & 1; dd[cb][e_] = mfma16(wd[((cb) >> 1) * 14 + 2 * (m_ % 7) + ((cb) & 1)], af[m_], dd[cb][e_]); PIN(dd[cb][e_]); } while (0)
      constexpr int HRS = 2;
      auto arrive = [&](auto Pi) AI {
        constexpr int p = decltype(Pi)::v, n = 2 * p;
        static_assert(af_cnt(p - 1) >= n + 2, "");
        VMW(af_cnt(p - 1) - (n + 2) + (p > HRS && n + 1 < af_cnt(HRS) ? 2 : 0) + (n + 1 < NAF) + (p == 0));
        pin_all<n, n + 2>(af);
        STM((missing<n, n + 2>(af)), 27);
        if (__builtin_expect(missing<n, n + 2>(af), 0)) {
#pragma unroll 1
          for (unsigned sp = 0; sp < SPIN_CAP; ++sp) {
            __builtin_amdgcn_s_sleep(RSLP);
            AF_LD(n); AF_LD(n + 1); VMW(0); pin_all<n, n + 2>(af);
            if constexpr (p == 13) asm volatile("hk_code_end:");
            if (!missing<n, n + 2>(af)) break;
          }
        }
      };
      arrive(IC<0>{});
      pin_all<0, 28 - NLDS>(wd);
      sfor<0, 14>([&](auto Pi) AI {
        constexpr int p = decltype(Pi)::v, n = 2 * p, a0 = af_cnt(p - 1);
        DM_(0, 0); DM_(1, 0);
        if constexpr (p == 5) WAITC(rb2, C_E, 4);
        if constexpr (a0 < af_cnt(p)) AF_LD(a0);
        SCHED();
        DM_(2, 0); DM_(3, 0);
        if constexpr (a0 < af_cnt(p)) AF_LD(a0 + 1);
        if constexpr (p == HRS) { const int so_ = OFF_H + (tR * 28 + 4 * jj) * 1024, v_ = hv_; const rsrc_t r_ = rsD; LDA2(hr[0], v_, r_, so_); LDA2(hr[1], v_, r_, so_ + 512); }
        if constexpr (p == 8) {
          PIN(hr[0]); PIN(hr[1]);
          s0 = f32x4{lo_f(hr[0].x), hi_f(hr[0].x), lo_f(hr[0].y), hi_f(hr[0].y)}; s1 = f32x4{lo_f(hr[1].x), hi_f(hr[1].x), lo_f(hr[1].y), hi_f(hr[1].y)};
          PIN(s0); PIN(s1);
        }
        SCHED();
        DM_(0, 1); DM_(1, 1);
        if constexpr (p < 13) arrive(IC<p + 1>{});
        SCHED();
        DM_(2, 1); DM_(3, 1);
        if constexpr (p == 6 || p == 13) {
#pragma unroll
          for (int cb = 0; cb < 4; ++cb) {
            *(LAS v4u*)(L + rw[cb] + (p == 13) * 32768) = v4u{pk2(dd[cb][0].x, dd[cb][0].y), pk2(dd[cb][0].z, dd[cb][0].w), pk2(dd[cb][1].x, dd[cb][1].y), pk2(dd[cb][1].z, dd[cb][1].w)};
            dd[cb][0] = f32x4{}; dd[cb][1] = f32x4{};
          }
          if (p == 6) ST(22);
        }
        if (p == 11) ST(23);
        FAIR(p, 2, 6, 10);
        SCHED();
      });
    }
    ST(24);
    bar();
    {
      v4u v[8];
#pragma unroll
      for (int p = 0; p < 8; ++p) v[p] = lds128(L, ra + (p >> 2) * 16384 + (p & 3) * 256);
      SCHED();
#pragma unroll
      for (int p = 0; p < 8; p += 2) { s0 = mfma16(sel[(p >> 1) & 1], v[p], s0); s1 = mfma16(sel[(p >> 1) & 1], v[p + 1], s1); }
      __builtin_amdgcn_raw_buffer_store_b128(v4u{pk2(s0.x, s0.y), pk2(s0.z, s0.w), pk2(s1.x, s1.y), pk2(s1.z, s1.w)}, ro_, ov, 0, AUX_SC1);
    }
  }
}
}

extern "C" void kernel_launch(void* const* d_in, const int* in_sizes, int n_in, void* d_out, int out_size, void* d_ws, size_t ws_size, hipStream_t stream) {
  using namespace hk;
  Args a;
  a.x = (const u16*)d_in[0]; a.wqkv = (const u16*)d_in[1]; a.wphi = (const u16*)d_in[2]; a.wo = (const u16*)d_in[3];
  a.wg = (const u16*)d_in[5]; a.wu = (const u16*)d_in[6]; a.wd = (const u16*)d_in[7];
  a.out = (u16*)d_out; a.ws = (char*)d_ws;
  (void)hipFuncSetAttribute((const void*)k_main, hipFuncAttributeMaxDynamicSharedMemorySize, LDS_BYTES);
  k_main<<<256, 512, LDS_BYTES, stream>>>(a);
}
```
